```python
import torch

def _softmax(x, axis=-1):
    return torch.softmax(x, dim=axis)
def _gelu(x):
    return torch.nn.functional.gelu(x, approximate="tanh")
def _silu(x):
    return torch.nn.functional.silu(x)
def _sigmoid(x):
    return torch.sigmoid(x)
def _layernorm(x, axis=-1, eps=1e-5):
    m = x.mean(dim=axis, keepdim=True)
    v = x.var(dim=axis, keepdim=True, unbiased=False)
    return (x - m) / torch.sqrt(v + eps)
def _scatter_add(tgt, idx, src, axis=0):
    return tgt.clone().index_add_(axis, idx, src)
def _segment_sum(src, seg, n):
    out = torch.zeros((n,) + src.shape[1:], dtype=src.dtype)
    return out.index_add_(0, seg, src)
def _pad(x, pw):
    flat = tuple(v for pair in reversed(pw) for v in pair)
    return torch.nn.functional.pad(x, flat)

kernel_name = 'standalone__mha_softmax__near_ridge'

def setup_inputs():

    _w = (lambda a: (lambda t: t.to(torch.bfloat16) if t.is_floating_point() else t)(torch.as_tensor(a)))
    B, H, Sq, Sk, D = 8, 32, 1600, 512, 128
    return {
        "q": _w((lambda *s: torch.randn(*s, dtype=torch.bfloat16) * 0.8)(B, H, Sq, D)),
        "k": _w((lambda *s: torch.randn(*s, dtype=torch.bfloat16) * 0.8)(B, H, Sk, D)),
        "v": _w((lambda *s: torch.randn(*s, dtype=torch.bfloat16) * 0.05)(B, H, Sk, D)),
    }

def reference(q, k, v):
    scale = q.shape[-1] ** -0.5
    scores = torch.einsum("bhqd,bhkd->bhqk", q, k) * scale
    p = _softmax(scores, axis=-1)
    return torch.einsum("bhqk,bhkd->bhqd", p, v)
```

```cpp
#include <hip/hip_runtime.h>
#include <hip/hip_bf16.h>
#include <cstdio>

using bf16 = __hip_bfloat16;
constexpr int   D = 128, NW = 8, QBLK = 32, KVBLK = 64;
constexpr float SCALE = 0.088388347648318440f;
#define ATTN_THR 8.f
constexpr float THR = ATTN_THR;
#define ATTN_LAYOUT 0
constexpr int LAYOUT = ATTN_LAYOUT;
#define ATTN_SDEPTH 2
constexpr int SDEPTH = ATTN_SDEPTH;
static_assert(SDEPTH == 1 || SDEPTH == 2, "SDEPTH is 1 or 2");
#define ATTN_B 8
#define ATTN_HQ 32
#define ATTN_HKV 32
#define ATTN_S 1600
#define ATTN_SKV 512
constexpr int B = ATTN_B, HQ = ATTN_HQ, HKV = ATTN_HKV, S = ATTN_S, SKV = ATTN_SKV;
constexpr int LDQ = LAYOUT ? HQ * D : D, LDK = LAYOUT ? HKV * D : D, LDO = LDQ;
static_assert(SKV % (2 * KVBLK) == 0 && HQ % HKV == 0, "S % 256 == 0, SKV % 128 == 0 (even tile count), HQ % HKV == 0");
constexpr size_t SHM_V = KVBLK * D * 2, SHM_K = KVBLK * D * 2, SHM_ATTN = 2 * SHM_V + 2 * SHM_K + NW * 64 * 4;

using bf16x8 = __attribute__((ext_vector_type(8))) short;
using s16x4  = __attribute__((ext_vector_type(4))) short;
using f32x16 = __attribute__((ext_vector_type(16))) float;
using f32x8  = __attribute__((ext_vector_type(8))) float;
using u32x4  = __attribute__((ext_vector_type(4))) unsigned;
#define KSWZ(row, colB) ((row) * 256 + ((colB) ^ (((row) & 7) << 4)))
#define SBAR() __builtin_amdgcn_sched_barrier(0)
__device__ __forceinline__ int crow(int r, int hi) { return (r & 3) + 8 * (r >> 2) + 4 * hi; }
__device__ __forceinline__ unsigned cvtpk(float lo, float hi) {
  unsigned r; asm volatile("v_cvt_pk_bf16_f32 %0, %1, %2" : "=v"(r) : "v"(lo), "v"(hi)); return r;
}
template <typename TIn> struct Stage;
template <> struct Stage<bf16>  { using T = bf16x8;
  __device__ static __forceinline__ T ld8(const bf16* p) { return *reinterpret_cast<const bf16x8*>(p); }
  __device__ static __forceinline__ bf16x8 tobf(T x) { return x; } };
template <> struct Stage<float> { using T = f32x8;
  __device__ static __forceinline__ T ld8(const float* p) { return *reinterpret_cast<const f32x8*>(p); }
  __device__ static __forceinline__ bf16x8 tobf(T x) {
    u32x4 w = {cvtpk(x[0], x[1]), cvtpk(x[2], x[3]), cvtpk(x[4], x[5]), cvtpk(x[6], x[7])}; return *reinterpret_cast<bf16x8*>(&w); } };

__device__ __forceinline__ void partialSM(f32x16& p0, f32x16& p1, float& m_reg, float& mn, float& alpha) {
  constexpr float C = SCALE * 1.4426950408889634f;
  float pmax = p0[0]; for (int r = 1; r < 16; ++r) pmax = fmaxf(pmax, p0[r]); for (int r = 0; r < 16; ++r) pmax = fmaxf(pmax, p1[r]);
  { auto rr = __builtin_amdgcn_permlane32_swap(__float_as_uint(pmax), __float_as_uint(pmax), false, false);
    pmax = fmaxf(__uint_as_float(rr[0]), __uint_as_float(rr[1])); }
  if (__builtin_expect(__all(pmax - m_reg <= THR / SCALE), 1)) { mn = m_reg; alpha = 1.f; }
  else { mn = fmaxf(m_reg, pmax); alpha = __builtin_amdgcn_exp2f((m_reg - mn) * C); m_reg = mn; }
  float mnC = -mn * C;
  for (int r = 0; r < 16; ++r) p0[r] = fmaf(p0[r], C, mnC); for (int r = 0; r < 16; ++r) p1[r] = fmaf(p1[r], C, mnC);
  for (int r = 0; r < 16; ++r) p0[r] = __builtin_amdgcn_exp2f(p0[r]);
}
__device__ __forceinline__ void finishSM(f32x16& p0, f32x16& p1, float alpha, float& l_reg, bf16x8& pa0, bf16x8& pa1, bf16x8& pa2, bf16x8& pa3) {
  for (int r = 0; r < 16; ++r) p1[r] = __builtin_amdgcn_exp2f(p1[r]);
  float ps = 0; for (int r = 0; r < 16; ++r) ps += p0[r]; for (int r = 0; r < 16; ++r) ps += p1[r];
  { auto rr = __builtin_amdgcn_permlane32_swap(__float_as_uint(ps), __float_as_uint(ps), false, false);
    ps = __uint_as_float(rr[0]) + __uint_as_float(rr[1]); }
  l_reg = l_reg * alpha + ps;
#define PK4(P, BASE, OUT) do { unsigned a0 = cvtpk(P[BASE + 0], P[BASE + 1]), a1 = cvtpk(P[BASE + 2], P[BASE + 3]);   \
    unsigned b0 = cvtpk(P[BASE + 4], P[BASE + 5]), b1 = cvtpk(P[BASE + 6], P[BASE + 7]);                              \
    auto r0 = __builtin_amdgcn_permlane32_swap(a0, b0, false, false); auto r1 = __builtin_amdgcn_permlane32_swap(a1, b1, false, false); \
    u32x4 w = {r0[0], r1[0], r0[1], r1[1]}; OUT = *reinterpret_cast<bf16x8*>(&w); } while (0)
  PK4(p0, 0, pa0); PK4(p0, 8, pa1); PK4(p1, 0, pa2); PK4(p1, 8, pa3);
#undef PK4
}
__device__ __forceinline__ void qkt(f32x16& p0, f32x16& p1, const bf16* Ks, const bf16x8* qr, int r32, int hi) {
  p0 = f32x16{}; p1 = f32x16{};
  for (int d0 = 0; d0 < 8; ++d0) { int cb = (d0 * 16 + hi * 8) * 2;
    bf16x8 b0 = *reinterpret_cast<const bf16x8*>((const char*)Ks + KSWZ(r32, cb));
    bf16x8 b1 = *reinterpret_cast<const bf16x8*>((const char*)Ks + KSWZ(32 + r32, cb));
    p0 = __builtin_amdgcn_mfma_f32_32x32x16_bf16(b0, qr[d0], p0, 0, 0, 0);
    p1 = __builtin_amdgcn_mfma_f32_32x32x16_bf16(b1, qr[d0], p1, 0, 0, 0); }
}
__device__ __forceinline__ int v_st(int k, int c) { const int kk = (k & ~0xC) | ((k & 4) << 1) | ((k & 8) >> 1); return ((kk >> 3) * 4 + (c >> 5)) * 512 + ((kk & 7) * 32 + (c & 31)) * 2; }
__device__ __forceinline__ int v_rd_base(int lane) { return ((lane & 3) << 3) | (((lane >> 2) & 3) << 6) | (((lane >> 4) & 1) << 5) | (((lane >> 5) & 1) << 8); }
constexpr int v_rd_off(int d0, int ks, int half) { return d0 * 512 + ks * 4096 + half * 2048; }
template <int OFF> __device__ __forceinline__ s16x4 tr_read(int vb) {
  s16x4 r; asm volatile("ds_read_b64_tr_b16 %0, %1 offset:%2" : "=&v"(r) : "v"(vb), "i"(OFF) : "memory"); return r;
}
template <int D0> __device__ __forceinline__ void pv_one(f32x16& od, int vb, bf16x8 pa0, bf16x8 pa1, bf16x8 pa2, bf16x8 pa3) {
  const s16x4 l0 = tr_read<v_rd_off(D0, 0, 0)>(vb), h0 = tr_read<v_rd_off(D0, 0, 1)>(vb), l1 = tr_read<v_rd_off(D0, 1, 0)>(vb), h1 = tr_read<v_rd_off(D0, 1, 1)>(vb);
  const s16x4 l2 = tr_read<v_rd_off(D0, 2, 0)>(vb), h2 = tr_read<v_rd_off(D0, 2, 1)>(vb), l3 = tr_read<v_rd_off(D0, 3, 0)>(vb), h3 = tr_read<v_rd_off(D0, 3, 1)>(vb);
  asm volatile("s_waitcnt lgkmcnt(0)" ::: "memory"); SBAR();
#define PK(L, H) (bf16x8){L[0], L[1], L[2], L[3], H[0], H[1], H[2], H[3]}
  od = __builtin_amdgcn_mfma_f32_32x32x16_bf16(pa0, PK(l0, h0), od, 0, 0, 0);
  od = __builtin_amdgcn_mfma_f32_32x32x16_bf16(pa1, PK(l1, h1), od, 0, 0, 0);
  od = __builtin_amdgcn_mfma_f32_32x32x16_bf16(pa2, PK(l2, h2), od, 0, 0, 0);
  od = __builtin_amdgcn_mfma_f32_32x32x16_bf16(pa3, PK(l3, h3), od, 0, 0, 0);
#undef PK
}
__device__ __forceinline__ void pv_d0(f32x16* o, int vb, bf16x8 pa0, bf16x8 pa1, bf16x8 pa2, bf16x8 pa3) {
  pv_one<0>(o[0], vb, pa0, pa1, pa2, pa3); pv_one<1>(o[1], vb, pa0, pa1, pa2, pa3); pv_one<2>(o[2], vb, pa0, pa1, pa2, pa3); pv_one<3>(o[3], vb, pa0, pa1, pa2, pa3);
}

template <typename TQ>
__device__ __forceinline__ void attn_dense_body(const TQ* __restrict__ Qb, const bf16* __restrict__ Kh, const bf16* __restrict__ Vh,
                                                bf16* __restrict__ Ob, int seq, char* lds, int nvalid) {
  using St = Stage<bf16>; using SQ = Stage<TQ>;
  const int tid = threadIdx.x, wid = tid >> 6, lane = tid & 63, r32 = lane & 31, hi = lane >> 5;
  bf16* V_lds = (bf16*)lds; bf16* K_lds = (bf16*)(lds + 2 * SHM_V);
  float* ws = (float*)(lds + 2 * SHM_V + 2 * SHM_K) + wid * 64; float* li_l = ws; float* al_l = ws + 32;
  float m_reg = -1e30f, l_reg = 0; f32x16 o[4] = {}; bf16x8 qr[8];
  const int qrow = (wid * QBLK + r32) < nvalid ? (wid * QBLK + r32) : 0;
  const TQ* Qw = Qb + (long)(qrow) * LDQ + hi * 8;
#pragma unroll
  for (int d0 = 0; d0 < 8; ++d0) qr[d0] = SQ::tobf(SQ::ld8(Qw + d0 * 16));
  const int sr = tid >> 4, sc = (tid & 15) * 8, vst0 = v_st(sr, sc), vst1 = v_st(32 + sr, sc);
  const int vb0 = (int)(uintptr_t)V_lds + v_rd_base(lane);
  struct { typename St::T vs0, vs1, ks0, ks1; } sr_[SDEPTH];
#define SLOAD(i, k0) do { sr_[i].vs0 = St::ld8(&Vh[(long)((k0) + sr) * LDK + sc]); sr_[i].vs1 = St::ld8(&Vh[(long)((k0) + 32 + sr) * LDK + sc]); \
    sr_[i].ks0 = St::ld8(&Kh[(long)((k0) + sr) * LDK + sc]); sr_[i].ks1 = St::ld8(&Kh[(long)((k0) + 32 + sr) * LDK + sc]); } while (0)
#define SWRITE(b, i) do { *(bf16x8*)((char*)V_lds + (b) * SHM_V + vst0) = St::tobf(sr_[i].vs0);          \
    *(bf16x8*)((char*)V_lds + (b) * SHM_V + vst1) = St::tobf(sr_[i].vs1); int kc = sc * 2;               \
    *(bf16x8*)((char*)K_lds + (b) * SHM_K + KSWZ(sr, kc)) = St::tobf(sr_[i].ks0);                       \
    *(bf16x8*)((char*)K_lds + (b) * SHM_K + KSWZ(32 + sr, kc)) = St::tobf(sr_[i].ks1); } while (0)
#define SWAIT() do { if constexpr (SDEPTH == 2) asm volatile("s_waitcnt vmcnt(4)" ::: "memory"); else asm volatile("s_waitcnt vmcnt(0)" ::: "memory"); } while (0)
#define RESC(a) do { if (__any((a) < 1.f)) { if (hi == 0) al_l[r32] = (a); asm volatile("s_waitcnt lgkmcnt(0)" ::: "memory"); \
    for (int d = 0; d < 4; ++d) for (int r = 0; r < 16; ++r) o[d][r] *= al_l[crow(r, hi)]; } } while (0)
  f32x16 pA0, pA1, pB0, pB1; float mnA, mnB, alA, alB; bf16x8 pa0, pa1, pa2, pa3; const int NT = seq / KVBLK;
  constexpr int SE = 0, SO = SDEPTH - 1;
  SLOAD(SE, 0); asm volatile("s_waitcnt vmcnt(0)" ::: "memory"); SWRITE(0, SE); __syncthreads();
  qkt(pA0, pA1, K_lds, qr, r32, hi); partialSM(pA0, pA1, m_reg, mnA, alA);
  SLOAD(SO, KVBLK); if constexpr (SDEPTH == 2) { if (2 < NT) SLOAD(SE, 2 * KVBLK); }
  SWAIT(); SWRITE(1, SO); __syncthreads();
  for (int j = 1; j + 1 < NT; j += 2) {
    SBAR(); qkt(pB0, pB1, (bf16*)((char*)K_lds + SHM_K), qr, r32, hi);
    finishSM(pA0, pA1, alA, l_reg, pa0, pa1, pa2, pa3); SBAR();
    SLOAD(SO, (j + SDEPTH) * KVBLK); SBAR();
    pv_d0(o, vb0, pa0, pa1, pa2, pa3); partialSM(pB0, pB1, m_reg, mnB, alB);
    __syncthreads(); SWAIT(); SWRITE(0, SE);
    RESC(alB); __syncthreads();
    SBAR(); qkt(pA0, pA1, K_lds, qr, r32, hi);
    finishSM(pB0, pB1, alB, l_reg, pa0, pa1, pa2, pa3); SBAR();
    if (SDEPTH == 1 || j + 3 < NT) SLOAD(SE, (j + 1 + SDEPTH) * KVBLK); SBAR();
    pv_d0(o, vb0 + (int)SHM_V, pa0, pa1, pa2, pa3); partialSM(pA0, pA1, m_reg, mnA, alA);
    __syncthreads(); SWAIT(); SWRITE(1, SO);
    RESC(alA); __syncthreads();
  }
  SBAR(); qkt(pB0, pB1, (bf16*)((char*)K_lds + SHM_K), qr, r32, hi);
  finishSM(pA0, pA1, alA, l_reg, pa0, pa1, pa2, pa3); SBAR();
  pv_d0(o, vb0, pa0, pa1, pa2, pa3); partialSM(pB0, pB1, m_reg, mnB, alB);
  __syncthreads(); RESC(alB);
  finishSM(pB0, pB1, alB, l_reg, pa0, pa1, pa2, pa3); SBAR();
  pv_d0(o, vb0 + (int)SHM_V, pa0, pa1, pa2, pa3);
  if (hi == 0) li_l[r32] = l_reg; asm volatile("s_waitcnt lgkmcnt(0)" ::: "memory");
  float rli[16];
#pragma unroll
  for (int r = 0; r < 16; ++r) rli[r] = __builtin_amdgcn_rcpf(li_l[crow(r, hi)]);
  bf16* Ow = Ob + (long)(wid * QBLK) * LDO;
  if (wid * QBLK < nvalid)
#pragma unroll
  for (int r = 0; r < 16; ++r) { int orow = crow(r, hi);
    for (int d0 = 0; d0 < 4; ++d0) Ow[(long)orow * LDO + d0 * 32 + r32] = __float2bfloat16(o[d0][r] * rli[r]); }
#undef SLOAD
#undef SWRITE
#undef SWAIT
#undef RESC
}

template <typename TQ>
__global__ __launch_bounds__(64 * NW, 1)
void attn_fwd_dense(const TQ* __restrict__ Q, const bf16* __restrict__ K, const bf16* __restrict__ V, bf16* __restrict__ O, int sq, int skv, int GQA) {
  extern __shared__ __attribute__((aligned(16))) char shm[];
  const long b = blockIdx.z, h = blockIdx.y, Hq = gridDim.y, Hkv = Hq / GQA, kvh = h / GQA, i0 = (long)blockIdx.x * (QBLK * NW);
  if constexpr (LAYOUT == 0) {
    const long q0 = ((b * Hq + h) * sq + i0) * D, k0 = (b * Hkv + kvh) * skv * (long)D;
    attn_dense_body<TQ>(Q + q0, K + k0, V + k0, O + q0, skv, shm, sq - (int)i0);
  } else {
    const long q0 = ((b * sq + i0) * HQ + h) * (long)D, k0 = (b * skv * HKV + kvh) * (long)D;
    attn_dense_body<TQ>(Q + q0, K + k0, V + k0, O + q0, skv, shm, sq - (int)i0);
  }
}

extern "C" void kernel_launch(void* const* d_in, const int* in_sizes, int n_in, void* d_out, int out_size, void* d_ws, size_t ws_size, hipStream_t stream) {
  hipFuncSetAttribute((const void*)attn_fwd_dense<bf16>, hipFuncAttributeMaxDynamicSharedMemorySize, (int)SHM_ATTN);
  hipLaunchKernelGGL(attn_fwd_dense<bf16>, dim3((S + QBLK * NW - 1) / (QBLK * NW), HQ, B), dim3(64 * NW), SHM_ATTN, stream,
                     (const bf16*)d_in[0], (const bf16*)d_in[1], (const bf16*)d_in[2], (bf16*)d_out, S, SKV, HQ / HKV);
}
```

```cpp
#include <hip/hip_runtime.h>
#include <hip/hip_bf16.h>

typedef short bf16x8 __attribute__((ext_vector_type(8)));
typedef __bf16 bfv8 __attribute__((ext_vector_type(8)));
typedef float f32x4 __attribute__((ext_vector_type(4)));
typedef float f32x2 __attribute__((ext_vector_type(2)));
typedef __bf16 bfv2 __attribute__((ext_vector_type(2)));
typedef unsigned u32x4 __attribute__((ext_vector_type(4)));
typedef unsigned u32x2 __attribute__((ext_vector_type(2)));

constexpr int SQ = 1600, SK = 512, HD = 128, NH = 256;

__device__ __forceinline__ unsigned pack2(float a, float b) {
  f32x2 v = {a, b};
  return __builtin_bit_cast(unsigned, __builtin_convertvector(v, bfv2));
}

__device__ __forceinline__ void slow_head(const unsigned short* __restrict__ Q, const unsigned short* __restrict__ K,
                                          const unsigned short* __restrict__ V, unsigned short* __restrict__ O, int wid) {
  const int lane = __builtin_amdgcn_mbcnt_hi(~0u, __builtin_amdgcn_mbcnt_lo(~0u, 0u));
  const int j = lane & 15, g = lane >> 4;
  const float c = 1.4426950408889634f * 0.08838834764831845f;
  for (int q0 = wid * 16; q0 < SQ; q0 += 64) {
    bfv8 qf[4];
#pragma unroll
    for (int ks = 0; ks < 4; ++ks) qf[ks] = *(const bfv8*)(Q + (q0 + j) * HD + 32 * ks + 8 * g);
    f32x4 o[8];
#pragma unroll
    for (int d = 0; d < 8; ++d) o[d] = f32x4{0.f, 0.f, 0.f, 0.f};
    float m = -INFINITY, l = 0.f;
    for (int k0 = 0; k0 < SK; k0 += 32) {
      f32x4 s[2];
#pragma unroll
      for (int h = 0; h < 2; ++h) {
        s[h] = f32x4{0.f, 0.f, 0.f, 0.f};
#pragma unroll
        for (int ks = 0; ks < 4; ++ks) {
          const bfv8 kf = *(const bfv8*)(K + (k0 + 16 * h + j) * HD + 32 * ks + 8 * g);
          s[h] = __builtin_amdgcn_mfma_f32_16x16x32_bf16(kf, qf[ks], s[h], 0, 0, 0);
        }
      }
      float mx = fmaxf(fmaxf(fmaxf(s[0][0], s[0][1]), fmaxf(s[0][2], s[0][3])), fmaxf(fmaxf(s[1][0], s[1][1]), fmaxf(s[1][2], s[1][3])));
      mx = fmaxf(mx, __shfl_xor(mx, 16));
      mx = fmaxf(mx, __shfl_xor(mx, 32));
      const float mn = fmaxf(m, mx);
      const float ms = (mn == -INFINITY) ? 0.f : mn;
      const float alpha = exp2f((m - ms) * c);
      m = mn;
      u32x4 pw;
      float ps = 0.f;
#pragma unroll
      for (int h = 0; h < 2; ++h)
#pragma unroll
        for (int r = 0; r < 2; ++r) {
          const unsigned w = pack2(exp2f((s[h][2 * r] - ms) * c), exp2f((s[h][2 * r + 1] - ms) * c));
          pw[2 * h + r] = w;
          ps += __uint_as_float(w << 16) + __uint_as_float(w & 0xffff0000u);
        }
      ps += __shfl_xor(ps, 16);
      ps += __shfl_xor(ps, 32);
      l = l * alpha + ps;
      const bfv8 pf = __builtin_bit_cast(bfv8, pw);
#pragma unroll
      for (int d = 0; d < 8; ++d) {
        bf16x8 vv;
#pragma unroll
        for (int e = 0; e < 8; ++e) vv[e] = (short)V[(k0 + 16 * (e >> 2) + 4 * g + (e & 3)) * HD + 16 * d + j];
        o[d] = __builtin_amdgcn_mfma_f32_16x16x32_bf16(__builtin_bit_cast(bfv8, vv), pf, o[d] * alpha, 0, 0, 0);
      }
    }
    const float il = 1.f / l;
#pragma unroll
    for (int d = 0; d < 8; ++d) {
      u32x2 w = {pack2(o[d][0] * il, o[d][1] * il), pack2(o[d][2] * il, o[d][3] * il)};
      *(u32x2*)(O + (q0 + j) * HD + 16 * d + 4 * g) = w;
    }
  }
}

__global__ __launch_bounds__(256, 1) void mha_fwd(const unsigned short* __restrict__ Q, const unsigned short* __restrict__ K,
                                                  const unsigned short* __restrict__ V, unsigned short* __restrict__ O) {
  extern __shared__ __attribute__((aligned(16))) char lds[];
  const int h = blockIdx.x;
  const int wid = __builtin_amdgcn_readfirstlane(threadIdx.x >> 6);
  const void* ka = (const void*)__builtin_amdgcn_kernarg_segment_ptr();
  int bad;
  asm volatile(
R"(.macro MA d,f,a,g,b
v_mfma_f32_16x16x32_bf16 v[\d:\d+3], \f[\a:\a+3], \g[\b:\b+3], v[\d:\d+3]
.endm
.macro MC d,f,a,g,b,c
v_mfma_f32_16x16x32_bf16 v[\d:\d+3], \f[\a:\a+3], \g[\b:\b+3], \c
.endm
.macro MR d,f,a,g,b,c
v_mfma_f32_16x16x32_bf16 v[\d:\d+3], \f[\a:\a+3], \g[\b:\b+3], v[\c:\c+3]
.endm
.macro EX r
v_exp_f32 v\r, v\r
.endm
.macro CV d,a,b
v_cvt_pk_bf16_f32 v\d, v\a, v\b
.endm
.macro MU d,a
v_mul_f32 v\d, v\d, v\a
.endm
.macro FM d,a,b
v_fma_f32 v\d, v\d, v\a, v\b
.endm
.macro RD f,d,a,o
ds_read_b128 \f[\d:\d+3], v\a offset:\o
.endm
.macro WR a,d,o
ds_write_b128 v\a, v[\d:\d+3] offset:\o
.endm
s_load_dwordx4 s[28:31], %[ka], 0x0
s_load_dwordx4 s[68:71], %[ka], 0x10
v_mbcnt_lo_u32_b32 v251, -1, 0
v_mbcnt_hi_u32_b32 v251, -1, v251
s_getpc_b64 s[20:21]
PCREF%=:
s_and_b32 s21, s21, 0xffff
s_mov_b32 s22, CEND%=-PCREF%=
s_mov_b32 s23, 0x20000
s_lshr_b32 s53, %[hid], 3
s_and_b32 s53, s53, 15
s_lshl_b32 s53, s53, 2
s_add_u32 s53, s53, %[wid]
s_lshl_b32 s24, s53, 10
s_mov_b32 s53, s24
v_lshlrev_b32 v254, 4, v251
buffer_load_dwordx4 v[0:3], v254, s[20:23], s53 offen
s_mov_b32 s54, %[wid]
v_lshrrev_b32 v252, 4, v251
v_and_b32 v253, 15, v251
s_lshl_b32 s65, s54, 13
s_mov_b32 s42, 0x20000
s_mov_b32 s43, 0x20000
s_mov_b32 s38, 0x64000
s_mov_b32 s39, 0x20000
s_mov_b32 s46, 0x20000
s_mov_b32 s47, 0x20000
s_mov_b32 s50, 0x64000
s_mov_b32 s51, 0x20000
s_mul_i32 s60, %[hid], 0x64000
s_lshl_b32 s61, %[hid], 17
v_lshlrev_b32 v248, 8, v253
v_lshl_add_u32 v248, v252, 4, v248
v_add_u32 v248, s65, v248
s_and_b32 s55, s54, 1
s_lshl_b32 s67, s55, 1
v_and_b32 v254, 3, v251
v_xor_b32 v254, s67, v254
v_lshlrev_b32 v237, 4, v254
v_lshrrev_b32 v254, 5, v251
v_lshl_add_u32 v237, v254, 6, v237
v_lshrrev_b32 v254, 2, v251
v_and_b32 v254, 7, v254
v_lshl_add_u32 v237, v254, 8, v237
s_lshl_b32 s67, s54, 11
v_add_u32 v237, s67, v237
s_lshr_b32 s67, s54, 1
s_lshl_b32 s67, s67, 12
s_lshl_b32 s55, s55, 10
s_add_u32 s64, s67, s55
v_lshlrev_b32 v254, 2, v252
v_xor_b32 v254, v253, v254
v_lshlrev_b32 v249, 4, v254
v_lshl_add_u32 v249, v252, 8, v249
v_add_u32 v249, s65, v249
s_waitcnt lgkmcnt(0)
s_add_u32 s40, s30, s61
s_addc_u32 s41, s31, 0
s_and_b32 s41, s41, 0xffff
s_mov_b32 s53, 0
buffer_load_dwordx4 v[96:99], v248, s[40:43], s53 offen offset:0 nt
s_mov_b32 s53, 0
buffer_load_dwordx4 v[100:103], v248, s[40:43], s53 offen offset:64 nt
s_mov_b32 s53, 0
buffer_load_dwordx4 v[104:107], v248, s[40:43], s53 offen offset:128 nt
s_mov_b32 s53, 0
buffer_load_dwordx4 v[108:111], v248, s[40:43], s53 offen offset:192 nt
s_mov_b32 s53, 0x1000
buffer_load_dwordx4 v[112:115], v248, s[40:43], s53 offen offset:0 nt
s_mov_b32 s53, 0x1000
buffer_load_dwordx4 v[116:119], v248, s[40:43], s53 offen offset:64 nt
s_mov_b32 s53, 0x1000
buffer_load_dwordx4 v[120:123], v248, s[40:43], s53 offen offset:128 nt
s_mov_b32 s53, 0x1000
buffer_load_dwordx4 v[124:127], v248, s[40:43], s53 offen offset:192 nt
s_add_u32 s36, s28, s60
s_addc_u32 s37, s29, 0
s_and_b32 s37, s37, 0xffff
s_add_u32 s44, s68, s61
s_addc_u32 s45, s69, 0
s_and_b32 s45, s45, 0xffff
s_add_u32 s48, s70, s60
s_addc_u32 s49, s71, 0
s_and_b32 s49, s49, 0xffff
s_add_u32 s55, s54, 0
s_and_b32 s55, s55, 3
s_lshl_b32 s56, s55, 13
s_add_u32 s55, s54, 1
s_and_b32 s55, s55, 3
s_lshl_b32 s57, s55, 13
s_add_u32 s55, s54, 2
s_and_b32 s55, s55, 3
s_lshl_b32 s58, s55, 13
s_add_u32 s55, s54, 3
s_and_b32 s55, s55, 3
s_lshl_b32 s59, s55, 13
s_mov_b32 s26, 0x3e0293ee
s_mov_b32 s27, 0x3e0293ee
v_lshrrev_b32 v254, 3, v253
v_lshlrev_b32 v236, 10, v254
v_lshlrev_b32 v254, 1, v254
v_xor_b32 v254, v252, v254
v_lshl_add_u32 v236, v254, 4, v236
v_and_b32 v254, 7, v253
v_lshl_add_u32 v236, v254, 6, v236
v_lshlrev_b32 v254, 4, v251
s_mov_b32 s67, 0x20000
s_lshr_b32 s55, s56, 3
s_add_u32 s55, s55, s67
v_add_u32 v238, s55, v254
s_lshr_b32 s55, s57, 3
s_add_u32 s55, s55, s67
v_add_u32 v239, s55, v254
s_lshr_b32 s55, s58, 3
s_add_u32 s55, s55, s67
v_add_u32 v240, s55, v254
s_lshr_b32 s55, s59, 3
s_add_u32 s55, s55, s67
v_add_u32 v241, s55, v254
s_lshr_b32 s55, s65, 3
s_add_u32 s55, s55, s67
s_sub_u32 s20, s20, s55
s_subb_u32 s21, s21, 0
s_add_u32 s22, s22, s55
v_lshrrev_b32 v254, 2, v251
v_lshlrev_b32 v250, 8, v254
v_and_b32 v254, 3, v254
v_xor_b32 v254, s54, v254
v_lshl_add_u32 v250, v254, 6, v250
v_and_b32 v254, 3, v251
v_lshl_add_u32 v250, v254, 4, v250
v_add_u32 v250, 0x10000, v250
s_mov_b32 s67, 0x26000
v_lshl_add_u32 v243, v253, 4, s67
v_lshrrev_b32 v254, 1, v252
v_lshl_add_u32 v243, v254, 8, v243
s_lshl_b32 s55, s54, 2
v_add_u32 v242, s55, v243
v_lshlrev_b32 v244, 8, v253
v_lshl_add_u32 v244, v252, 4, v244
s_lshl_b32 s55, s54, 6
v_add_u32 v244, s55, v244
v_add_u32 v245, 0x1000, v244
v_mov_b32 v254, 0x3f803f80
v_and_b32 v255, 10, v253
v_cmp_gt_u32 vcc, 1, v255
s_nop 1
v_cndmask_b32 v220, 0, v254, vcc
v_xor_b32 v255, 8, v255
v_cmp_gt_u32 vcc, 1, v255
s_nop 1
v_cndmask_b32 v224, 0, v254, vcc
v_mov_b32 v221, v220
v_mov_b32 v225, v224
v_mov_b32 v222, v220
v_mov_b32 v226, v224
v_mov_b32 v223, v220
v_mov_b32 v227, v224
v_and_b32 v255, 1, v253
v_cmp_gt_u32 vcc, 1, v255
v_mov_b32 v254, 0x44444444
v_mov_b32 v255, 0xeeeeeeee
v_cndmask_b32 v234, v255, v254, vcc
v_mov_b32 v246, 1.0
s_mov_b32 s69, 0x5d800000
s_add_u32 m0, s64, 0
s_mov_b32 s53, 0
s_waitcnt vmcnt(7)
buffer_load_dwordx4 v237, s[36:39], s53 offen offset:0 lds
s_add_u32 m0, s64, 0x780
s_mov_b32 s53, 0
s_waitcnt vmcnt(7)
buffer_load_dwordx4 v237, s[36:39], s53 offen offset:128 lds
s_add_u32 m0, s64, 0x2000
s_mov_b32 s53, 0x2000
s_waitcnt vmcnt(7)
buffer_load_dwordx4 v237, s[36:39], s53 offen offset:0 lds
v_lshlrev_b32 v252, 16, v96
v_lshlrev_b32 v254, 16, v97
v_and_b32 v253, 0xffff0000, v96
v_and_b32 v255, 0xffff0000, v97
v_pk_mul_f32 v[252:253], v[252:253], s[26:27]
v_pk_mul_f32 v[254:255], v[254:255], s[26:27]
CV 96,252,253
CV 97,254,255
v_accvgpr_write_b32 a0, v96
v_accvgpr_write_b32 a1, v97
v_lshlrev_b32 v252, 16, v98
v_lshlrev_b32 v254, 16, v99
v_and_b32 v253, 0xffff0000, v98
v_and_b32 v255, 0xffff0000, v99
v_pk_mul_f32 v[252:253], v[252:253], s[26:27]
v_pk_mul_f32 v[254:255], v[254:255], s[26:27]
CV 98,252,253
CV 99,254,255
v_accvgpr_write_b32 a2, v98
v_accvgpr_write_b32 a3, v99
v_lshlrev_b32 v252, 16, v100
v_lshlrev_b32 v254, 16, v101
v_and_b32 v253, 0xffff0000, v100
v_and_b32 v255, 0xffff0000, v101
v_pk_mul_f32 v[252:253], v[252:253], s[26:27]
v_pk_mul_f32 v[254:255], v[254:255], s[26:27]
CV 100,252,253
CV 101,254,255
v_accvgpr_write_b32 a4, v100
v_accvgpr_write_b32 a5, v101
v_lshlrev_b32 v252, 16, v102
v_lshlrev_b32 v254, 16, v103
v_and_b32 v253, 0xffff0000, v102
v_and_b32 v255, 0xffff0000, v103
v_pk_mul_f32 v[252:253], v[252:253], s[26:27]
v_pk_mul_f32 v[254:255], v[254:255], s[26:27]
CV 102,252,253
CV 103,254,255
v_accvgpr_write_b32 a6, v102
v_accvgpr_write_b32 a7, v103
v_lshlrev_b32 v252, 16, v104
v_lshlrev_b32 v254, 16, v105
v_and_b32 v253, 0xffff0000, v104
v_and_b32 v255, 0xffff0000, v105
v_pk_mul_f32 v[252:253], v[252:253], s[26:27]
v_pk_mul_f32 v[254:255], v[254:255], s[26:27]
CV 104,252,253
CV 105,254,255
v_accvgpr_write_b32 a8, v104
v_accvgpr_write_b32 a9, v105
v_lshlrev_b32 v252, 16, v106
v_lshlrev_b32 v254, 16, v107
v_and_b32 v253, 0xffff0000, v106
v_and_b32 v255, 0xffff0000, v107
v_pk_mul_f32 v[252:253], v[252:253], s[26:27]
v_pk_mul_f32 v[254:255], v[254:255], s[26:27]
CV 106,252,253
CV 107,254,255
v_accvgpr_write_b32 a10, v106
v_accvgpr_write_b32 a11, v107
s_waitcnt vmcnt(7)
v_lshlrev_b32 v252, 16, v108
v_lshlrev_b32 v254, 16, v109
v_and_b32 v253, 0xffff0000, v108
v_and_b32 v255, 0xffff0000, v109
v_pk_mul_f32 v[252:253], v[252:253], s[26:27]
v_pk_mul_f32 v[254:255], v[254:255], s[26:27]
CV 108,252,253
CV 109,254,255
v_accvgpr_write_b32 a12, v108
v_accvgpr_write_b32 a13, v109
v_lshlrev_b32 v252, 16, v110
v_lshlrev_b32 v254, 16, v111
v_and_b32 v253, 0xffff0000, v110
v_and_b32 v255, 0xffff0000, v111
v_pk_mul_f32 v[252:253], v[252:253], s[26:27]
v_pk_mul_f32 v[254:255], v[254:255], s[26:27]
CV 110,252,253
CV 111,254,255
v_accvgpr_write_b32 a14, v110
v_accvgpr_write_b32 a15, v111
s_add_u32 m0, s64, 0x2780
s_mov_b32 s53, 0x2000
buffer_load_dwordx4 v237, s[36:39], s53 offen offset:128 lds
s_add_u32 m0, s65, 0x10000
s_mov_b32 s53, 0
s_waitcnt vmcnt(7)
buffer_load_dwordx4 v249, s[44:47], s53 offen offset:0 nt lds
s_add_u32 m0, s65, 0x10400
s_mov_b32 s53, 0x400
s_waitcnt vmcnt(7)
buffer_load_dwordx4 v249, s[44:47], s53 offen offset:0 nt lds
s_add_u32 m0, s65, 0x10800
s_mov_b32 s53, 0x800
s_waitcnt vmcnt(7)
buffer_load_dwordx4 v249, s[44:47], s53 offen offset:0 nt lds
v_lshlrev_b32 v252, 16, v112
v_lshlrev_b32 v254, 16, v113
v_and_b32 v253, 0xffff0000, v112
v_and_b32 v255, 0xffff0000, v113
v_pk_mul_f32 v[252:253], v[252:253], s[26:27]
v_pk_mul_f32 v[254:255], v[254:255], s[26:27]
CV 112,252,253
CV 113,254,255
v_accvgpr_write_b32 a16, v112
v_accvgpr_write_b32 a17, v113
v_lshlrev_b32 v252, 16, v114
v_lshlrev_b32 v254, 16, v115
v_and_b32 v253, 0xffff0000, v114
v_and_b32 v255, 0xffff0000, v115
v_pk_mul_f32 v[252:253], v[252:253], s[26:27]
v_pk_mul_f32 v[254:255], v[254:255], s[26:27]
CV 114,252,253
CV 115,254,255
v_accvgpr_write_b32 a18, v114
v_accvgpr_write_b32 a19, v115
v_lshlrev_b32 v252, 16, v116
v_lshlrev_b32 v254, 16, v117
v_and_b32 v253, 0xffff0000, v116
v_and_b32 v255, 0xffff0000, v117
v_pk_mul_f32 v[252:253], v[252:253], s[26:27]
v_pk_mul_f32 v[254:255], v[254:255], s[26:27]
CV 116,252,253
CV 117,254,255
v_accvgpr_write_b32 a20, v116
v_accvgpr_write_b32 a21, v117
v_lshlrev_b32 v252, 16, v118
v_lshlrev_b32 v254, 16, v119
v_and_b32 v253, 0xffff0000, v118
v_and_b32 v255, 0xffff0000, v119
v_pk_mul_f32 v[252:253], v[252:253], s[26:27]
v_pk_mul_f32 v[254:255], v[254:255], s[26:27]
CV 118,252,253
CV 119,254,255
v_accvgpr_write_b32 a22, v118
v_accvgpr_write_b32 a23, v119
v_lshlrev_b32 v252, 16, v120
v_lshlrev_b32 v254, 16, v121
v_and_b32 v253, 0xffff0000, v120
v_and_b32 v255, 0xffff0000, v121
v_pk_mul_f32 v[252:253], v[252:253], s[26:27]
v_pk_mul_f32 v[254:255], v[254:255], s[26:27]
CV 120,252,253
CV 121,254,255
v_accvgpr_write_b32 a24, v120
v_accvgpr_write_b32 a25, v121
v_lshlrev_b32 v252, 16, v122
v_lshlrev_b32 v254, 16, v123
v_and_b32 v253, 0xffff0000, v122
v_and_b32 v255, 0xffff0000, v123
v_pk_mul_f32 v[252:253], v[252:253], s[26:27]
v_pk_mul_f32 v[254:255], v[254:255], s[26:27]
CV 122,252,253
CV 123,254,255
v_accvgpr_write_b32 a26, v122
v_accvgpr_write_b32 a27, v123
s_waitcnt vmcnt(7)
v_lshlrev_b32 v252, 16, v124
v_lshlrev_b32 v254, 16, v125
v_and_b32 v253, 0xffff0000, v124
v_and_b32 v255, 0xffff0000, v125
v_pk_mul_f32 v[252:253], v[252:253], s[26:27]
v_pk_mul_f32 v[254:255], v[254:255], s[26:27]
CV 124,252,253
CV 125,254,255
v_accvgpr_write_b32 a28, v124
v_accvgpr_write_b32 a29, v125
v_lshlrev_b32 v252, 16, v126
v_lshlrev_b32 v254, 16, v127
v_and_b32 v253, 0xffff0000, v126
v_and_b32 v255, 0xffff0000, v127
v_pk_mul_f32 v[252:253], v[252:253], s[26:27]
v_pk_mul_f32 v[254:255], v[254:255], s[26:27]
CV 126,252,253
CV 127,254,255
v_accvgpr_write_b32 a30, v126
v_accvgpr_write_b32 a31, v127
s_add_u32 m0, s65, 0x10c00
s_mov_b32 s53, 0xc00
buffer_load_dwordx4 v249, s[44:47], s53 offen offset:0 nt lds
s_add_u32 m0, s65, 0x11000
s_mov_b32 s53, 0x1000
s_waitcnt vmcnt(7)
buffer_load_dwordx4 v249, s[44:47], s53 offen offset:0 nt lds
s_add_u32 m0, s65, 0x11400
s_mov_b32 s53, 0x1400
s_waitcnt vmcnt(7)
buffer_load_dwordx4 v249, s[44:47], s53 offen offset:0 nt lds
s_add_u32 m0, s65, 0x11800
s_mov_b32 s53, 0x1800
s_waitcnt vmcnt(7)
buffer_load_dwordx4 v249, s[44:47], s53 offen offset:0 nt lds
s_waitcnt vmcnt(7)
s_barrier
RD a,64,236,0
RD a,68,236,512
)"
R"(RD a,72,236,2048
RD a,76,236,2560
RD a,80,236,4096
RD a,84,236,4608
RD a,88,236,6144
RD a,92,236,6656
s_add_u32 m0, s65, 0x11c00
s_mov_b32 s53, 0x1c00
buffer_load_dwordx4 v249, s[44:47], s53 offen offset:0 nt lds
s_add_u32 m0, s64, 0x4000
s_mov_b32 s53, 0x4000
s_waitcnt vmcnt(7)
buffer_load_dwordx4 v237, s[36:39], s53 offen offset:0 lds
s_add_u32 m0, s64, 0x4780
s_mov_b32 s53, 0x4000
s_waitcnt vmcnt(7)
buffer_load_dwordx4 v237, s[36:39], s53 offen offset:128 lds
s_add_u32 m0, s64, 0x6000
s_mov_b32 s53, 0x6000
s_waitcnt vmcnt(7)
buffer_load_dwordx4 v237, s[36:39], s53 offen offset:0 lds
s_add_u32 m0, s64, 0x6780
s_mov_b32 s53, 0x6000
s_waitcnt vmcnt(7)
buffer_load_dwordx4 v237, s[36:39], s53 offen offset:128 lds
s_add_u32 m0, s64, 0x8000
s_mov_b32 s53, 0x8000
s_waitcnt vmcnt(7)
buffer_load_dwordx4 v237, s[36:39], s53 offen offset:0 lds
s_add_u32 m0, s64, 0x8780
s_mov_b32 s53, 0x8000
s_waitcnt vmcnt(7)
buffer_load_dwordx4 v237, s[36:39], s53 offen offset:128 lds
s_add_u32 m0, s64, 0xa000
s_mov_b32 s53, 0xa000
s_waitcnt vmcnt(7)
buffer_load_dwordx4 v237, s[36:39], s53 offen offset:0 lds
s_waitcnt lgkmcnt(7)
MC 128,a,0,a,64,-4.0
s_waitcnt lgkmcnt(6)
MA 128,a,4,a,68
s_waitcnt lgkmcnt(5)
MA 128,a,8,a,72
s_waitcnt lgkmcnt(4)
MA 128,a,12,a,76
s_add_u32 m0, s64, 0xa780
s_waitcnt vmcnt(7)
s_mov_b32 s53, 0xa000
buffer_load_dwordx4 v237, s[36:39], s53 offen offset:128 lds
MC 132,a,16,a,64,-4.0
s_nop 3
EX 128
MA 132,a,20,a,68
EX 129
MA 132,a,24,a,72
EX 130
MA 132,a,28,a,76
EX 131
CV 176,128,129
CV 177,130,131
RD a,64,236,8192
RD a,68,236,8704
s_waitcnt lgkmcnt(5)
RD a,72,236,10240
MC 136,a,0,a,80,-4.0
s_waitcnt lgkmcnt(5)
s_nop 0
EX 132
MA 136,a,4,a,84
s_waitcnt lgkmcnt(4)
EX 133
MA 136,a,8,a,88
s_waitcnt lgkmcnt(3)
EX 134
MA 136,a,12,a,92
EX 135
MC 140,a,16,a,80,-4.0
CV 178,132,133
CV 179,134,135
MA 140,a,20,a,84
WR 238,176,8192
MA 140,a,24,a,88
s_mov_b32 s53, 0x8000
MA 140,a,28,a,92
s_waitcnt vmcnt(7)
buffer_load_dwordx4 v[144:147], v248, s[40:43], s53 offen offset:0 nt
EX 136
s_waitcnt vmcnt(7)
s_barrier
EX 137
EX 138
EX 139
RD a,76,236,10752
RD a,80,236,12288
CV 184,136,137
CV 185,138,139
RD a,84,236,12800
s_waitcnt lgkmcnt(6)
RD a,88,236,14336
RD a,92,236,14848
MC 128,a,0,a,64,-4.0
s_waitcnt lgkmcnt(7)
EX 140
MA 128,a,4,a,68
s_waitcnt lgkmcnt(6)
EX 141
MA 128,a,8,a,72
s_waitcnt lgkmcnt(4)
EX 142
MA 128,a,12,a,76
EX 143
CV 186,140,141
CV 187,142,143
WR 238,184,0
s_mov_b32 s53, 0x8000
buffer_load_dwordx4 v[148:151], v248, s[40:43], s53 offen offset:64 nt
MC 132,a,16,a,64,-4.0
s_nop 0
EX 128
MA 132,a,20,a,68
EX 129
MA 132,a,24,a,72
EX 130
MA 132,a,28,a,76
EX 131
CV 180,128,129
CV 181,130,131
RD a,64,236,16384
RD a,68,236,16896
s_waitcnt lgkmcnt(6)
RD a,72,236,18432
MC 136,a,0,a,80,-4.0
s_waitcnt lgkmcnt(6)
s_nop 0
EX 132
MA 136,a,4,a,84
s_waitcnt lgkmcnt(5)
EX 133
MA 136,a,8,a,88
s_waitcnt lgkmcnt(4)
EX 134
MA 136,a,12,a,92
EX 135
MC 140,a,16,a,80,-4.0
CV 182,132,133
CV 183,134,135
MA 140,a,20,a,84
WR 238,180,12288
MA 140,a,24,a,88
s_mov_b32 s53, 0x8000
MA 140,a,28,a,92
s_waitcnt vmcnt(7)
buffer_load_dwordx4 v[152:155], v248, s[40:43], s53 offen offset:128 nt
MC 216,v,220,v,176,0
EX 136
MA 216,v,224,v,184
s_waitcnt vmcnt(7) lgkmcnt(4)
s_barrier
EX 137
EX 138
EX 139
RD a,76,236,18944
RD a,80,236,20480
CV 188,136,137
CV 189,138,139
RD v,192,239,8192
RD v,196,240,8192
v_mov_b32 v228, v216
RD v,200,241,8192
RD a,84,236,20992
RD a,88,236,22528
RD a,92,236,23040
s_barrier
v_add_u32 v252, s56, v250
v_add_u32 v253, s57, v250
v_add_u32 v254, s58, v250
v_add_u32 v255, s59, v250
ds_read_b64_tr_b16 a[128:129], v252 offset:0
ds_read_b64_tr_b16 a[130:131], v252 offset:4096
ds_read_b64_tr_b16 a[136:137], v253 offset:0
s_waitcnt lgkmcnt(14)
ds_read_b64_tr_b16 a[138:139], v253 offset:4096
s_waitcnt lgkmcnt(14)
ds_read_b64_tr_b16 a[144:145], v254 offset:0
s_waitcnt lgkmcnt(14)
ds_read_b64_tr_b16 a[146:147], v254 offset:4096
s_waitcnt lgkmcnt(14)
ds_read_b64_tr_b16 a[152:153], v255 offset:0
s_waitcnt lgkmcnt(14)
ds_read_b64_tr_b16 a[154:155], v255 offset:4096
s_waitcnt lgkmcnt(14)
ds_read_b64_tr_b16 a[132:133], v252 offset:8
s_waitcnt lgkmcnt(14)
ds_read_b64_tr_b16 a[134:135], v252 offset:4104
s_waitcnt lgkmcnt(14)
ds_read_b64_tr_b16 a[140:141], v253 offset:8
s_waitcnt lgkmcnt(14)
ds_read_b64_tr_b16 a[142:143], v253 offset:4104
s_waitcnt lgkmcnt(14)
ds_read_b64_tr_b16 a[148:149], v254 offset:8
s_waitcnt lgkmcnt(14)
ds_read_b64_tr_b16 a[150:151], v254 offset:4104
s_waitcnt lgkmcnt(14)
ds_read_b64_tr_b16 a[156:157], v255 offset:8
s_waitcnt lgkmcnt(14)
ds_read_b64_tr_b16 a[158:159], v255 offset:4104
MC 128,a,0,a,64,-4.0
EX 140
MA 128,a,4,a,68
EX 141
MA 128,a,8,a,72
EX 142
MA 128,a,12,a,76
s_waitcnt lgkmcnt(14)
EX 143
MC 0,a,128,v,176,0
s_waitcnt lgkmcnt(12)
CV 190,140,141
CV 191,142,143
MA 0,a,136,v,192
s_waitcnt lgkmcnt(10)
WR 238,188,4096
MA 0,a,144,v,196
s_waitcnt lgkmcnt(9)
s_mov_b32 s53, 0x8000
MA 0,a,152,v,200
buffer_load_dwordx4 v[156:159], v248, s[40:43], s53 offen offset:192 nt
MC 132,a,16,a,64,-4.0
EX 128
MA 132,a,20,a,68
EX 129
MA 132,a,24,a,72
EX 130
MA 132,a,28,a,76
s_waitcnt lgkmcnt(7)
EX 131
MC 4,a,132,v,176,0
s_waitcnt lgkmcnt(5)
CV 176,128,129
CV 177,130,131
MA 4,a,140,v,192
s_waitcnt lgkmcnt(3)
RD a,64,236,24576
RD a,68,236,25088
MA 4,a,148,v,196
s_waitcnt lgkmcnt(3)
RD v,204,239,0
RD v,208,240,0
MA 4,a,156,v,200
RD v,212,241,0
RD a,72,236,26624
MC 136,a,0,a,80,-4.0
EX 132
MA 136,a,4,a,84
EX 133
MA 136,a,8,a,88
EX 134
MA 136,a,12,a,92
EX 135
MC 140,a,16,a,80,-4.0
CV 178,132,133
CV 179,134,135
MA 140,a,20,a,84
WR 238,176,16384
MA 140,a,24,a,88
s_mov_b32 s53, 0x9000
MA 140,a,28,a,92
s_waitcnt vmcnt(7)
buffer_load_dwordx4 v[160:163], v248, s[40:43], s53 offen offset:0 nt
MC 216,v,220,v,180,0
EX 136
MA 216,v,224,v,188
s_waitcnt vmcnt(7) lgkmcnt(2)
s_barrier
MC 8,a,128,v,184,0
EX 137
MA 8,a,136,v,204
EX 138
MA 8,a,144,v,208
EX 139
MA 8,a,152,v,212
RD a,76,236,27136
RD a,80,236,28672
MC 12,a,132,v,184,0
CV 184,136,137
CV 185,138,139
MA 12,a,140,v,204
RD v,192,239,12288
RD v,196,240,12288
v_mov_b32 v229, v216
MA 12,a,148,v,208
RD v,200,241,12288
RD a,84,236,29184
MA 12,a,156,v,212
RD a,88,236,30720
RD a,92,236,31232
v_accvgpr_read_b32 v253, a128
v_accvgpr_read_b32 v254, a132
v_and_b32 v251, 0x7fff7fff, v253
v_and_b32 v252, 0x7fff7fff, v254
v_accvgpr_read_b32 v253, a129
v_accvgpr_read_b32 v254, a133
v_and_b32 v253, 0x7fff7fff, v253
v_and_b32 v254, 0x7fff7fff, v254
v_pk_max_u16 v251, v251, v253
v_pk_max_u16 v252, v252, v254
v_accvgpr_read_b32 v253, a130
v_accvgpr_read_b32 v254, a134
v_and_b32 v253, 0x7fff7fff, v253
v_and_b32 v254, 0x7fff7fff, v254
v_pk_max_u16 v251, v251, v253
v_pk_max_u16 v252, v252, v254
v_accvgpr_read_b32 v253, a131
v_accvgpr_read_b32 v254, a135
v_and_b32 v253, 0x7fff7fff, v253
v_and_b32 v254, 0x7fff7fff, v254
v_pk_max_u16 v251, v251, v253
v_pk_max_u16 v252, v252, v254
v_lshrrev_b32 v253, 16, v251
v_lshrrev_b32 v254, 16, v252
v_and_b32 v251, 0xffff, v251
v_and_b32 v252, 0xffff, v252
v_max_u32 v251, v251, v253
v_max_u32 v252, v252, v254
v_lshl_or_b32 v251, v252, 16, v251
v_mov_b32 v253, v251
s_nop 1
v_permlane32_swap_b32 v251, v253
v_pk_max_u16 v251, v251, v253
v_mov_b32 v253, v251
s_nop 1
v_permlane16_swap_b32 v251, v253
v_pk_max_u16 v251, v251, v253
v_lshrrev_b32 v253, 16, v251
v_and_b32 v251, 0xffff, v251
v_min_u32 v251, v251, v253
s_nop 1
v_min_u32_dpp v251, v251, v251 row_ror:8 row_mask:0xf bank_mask:0xf
s_nop 1
v_min_u32_dpp v251, v251, v251 row_ror:4 row_mask:0xf bank_mask:0xf
s_nop 1
v_min_u32_dpp v251, v251, v251 row_ror:2 row_mask:0xf bank_mask:0xf
s_nop 1
v_min_u32_dpp v251, v251, v251 row_ror:1 row_mask:0xf bank_mask:0xf
v_min_u32 v251, 0x6f80, v251
v_and_b32 v251, 0x7f80, v251
v_add_u32 v251, 0x2c0, v251
v_lshlrev_b32 v235, 16, v251
MC 128,a,0,a,64,-4.0
EX 140
MA 128,a,4,a,68
s_waitcnt lgkmcnt(9)
EX 141
MA 128,a,8,a,72
s_waitcnt lgkmcnt(7)
EX 142
MA 128,a,12,a,76
EX 143
MC 16,a,128,v,180,0
s_waitcnt lgkmcnt(4)
CV 186,140,141
CV 187,142,143
MA 16,a,136,v,192
WR 238,184,0
MA 16,a,144,v,196
s_waitcnt lgkmcnt(4)
s_mov_b32 s53, 0x9000
MA 16,a,152,v,200
buffer_load_dwordx4 v[164:167], v248, s[40:43], s53 offen offset:64 nt
MC 132,a,16,a,64,-4.0
EX 128
MA 132,a,20,a,68
EX 129
MA 132,a,24,a,72
EX 130
MA 132,a,28,a,76
EX 131
MC 20,a,132,v,180,0
CV 180,128,129
CV 181,130,131
MA 20,a,140,v,192
RD a,64,236,32768
RD a,68,236,33280
MA 20,a,148,v,196
RD v,204,239,4096
RD v,208,240,4096
MA 20,a,156,v,200
RD v,212,241,4096
RD a,72,236,34816
MC 136,a,0,a,80,-4.0
s_waitcnt lgkmcnt(9)
EX 132
MA 136,a,4,a,84
s_waitcnt lgkmcnt(8)
EX 133
MA 136,a,8,a,88
s_waitcnt lgkmcnt(7)
EX 134
MA 136,a,12,a,92
EX 135
)"
R"(MC 140,a,16,a,80,-4.0
CV 182,132,133
CV 183,134,135
MA 140,a,20,a,84
WR 238,180,20480
MA 140,a,24,a,88
s_mov_b32 s53, 0x9000
MA 140,a,28,a,92
s_waitcnt vmcnt(7)
buffer_load_dwordx4 v[168:171], v248, s[40:43], s53 offen offset:128 nt
MC 216,v,220,v,176,0
EX 136
MA 216,v,224,v,184
s_waitcnt vmcnt(7) lgkmcnt(2)
s_barrier
MC 24,a,128,v,188,0
EX 137
MA 24,a,136,v,204
EX 138
MA 24,a,144,v,208
EX 139
MA 24,a,152,v,212
RD a,76,236,35328
RD a,80,236,36864
MC 28,a,132,v,188,0
CV 188,136,137
CV 189,138,139
MA 28,a,140,v,204
RD v,192,239,16384
RD v,196,240,16384
v_mov_b32 v230, v216
MA 28,a,148,v,208
RD v,200,241,16384
RD a,84,236,37376
MA 28,a,156,v,212
RD a,88,236,38912
RD a,92,236,39424
MC 128,a,0,a,64,-4.0
EX 140
MA 128,a,4,a,68
s_waitcnt lgkmcnt(9)
EX 141
MA 128,a,8,a,72
s_waitcnt lgkmcnt(7)
EX 142
MA 128,a,12,a,76
EX 143
MC 32,a,128,v,176,0
s_waitcnt lgkmcnt(4)
CV 190,140,141
CV 191,142,143
MA 32,a,136,v,192
WR 238,188,4096
MA 32,a,144,v,196
s_waitcnt lgkmcnt(4)
s_mov_b32 s53, 0x9000
MA 32,a,152,v,200
buffer_load_dwordx4 v[172:175], v248, s[40:43], s53 offen offset:192 nt
MC 132,a,16,a,64,-4.0
EX 128
MA 132,a,20,a,68
EX 129
MA 132,a,24,a,72
EX 130
MA 132,a,28,a,76
EX 131
MC 36,a,132,v,176,0
CV 176,128,129
CV 177,130,131
MA 36,a,140,v,192
RD a,64,236,40960
RD a,68,236,41472
MA 36,a,148,v,196
RD v,204,239,0
RD v,208,240,0
MA 36,a,156,v,200
RD v,212,241,0
RD a,72,236,43008
MC 136,a,0,a,80,-4.0
s_waitcnt lgkmcnt(9)
EX 132
MA 136,a,4,a,84
s_waitcnt lgkmcnt(8)
EX 133
MA 136,a,8,a,88
s_waitcnt lgkmcnt(7)
EX 134
MA 136,a,12,a,92
EX 135
MC 140,a,16,a,80,-4.0
CV 178,132,133
CV 179,134,135
MA 140,a,20,a,84
WR 238,176,8192
MA 140,a,24,a,88
s_add_u32 m0, s65, 0x18000
MA 140,a,28,a,92
s_waitcnt vmcnt(7)
s_mov_b32 s53, 0x8000
buffer_load_dwordx4 v249, s[44:47], s53 offen offset:0 nt lds
MC 216,v,220,v,180,0
EX 136
MA 216,v,224,v,188
s_waitcnt lgkmcnt(2)
s_barrier
MC 40,a,128,v,184,0
EX 137
MA 40,a,136,v,204
EX 138
MA 40,a,144,v,208
EX 139
MA 40,a,152,v,212
RD a,76,236,43520
RD a,80,236,45056
MC 44,a,132,v,184,0
CV 184,136,137
CV 185,138,139
MA 44,a,140,v,204
RD v,192,239,20480
RD v,196,240,20480
v_mov_b32 v231, v216
MA 44,a,148,v,208
RD v,200,241,20480
RD a,84,236,45568
MA 44,a,156,v,212
RD a,88,236,47104
RD a,92,236,47616
MC 128,a,0,a,64,-4.0
EX 140
MA 128,a,4,a,68
s_waitcnt lgkmcnt(9)
EX 141
MA 128,a,8,a,72
s_waitcnt lgkmcnt(7)
EX 142
MA 128,a,12,a,76
EX 143
MC 48,a,128,v,180,0
s_waitcnt lgkmcnt(4)
CV 186,140,141
CV 187,142,143
MA 48,a,136,v,192
WR 238,184,0
MA 48,a,144,v,196
s_waitcnt lgkmcnt(4)
s_add_u32 m0, s65, 0x18400
MA 48,a,152,v,200
s_waitcnt vmcnt(7)
s_mov_b32 s53, 0x8400
buffer_load_dwordx4 v249, s[44:47], s53 offen offset:0 nt lds
MC 132,a,16,a,64,-4.0
EX 128
MA 132,a,20,a,68
EX 129
MA 132,a,24,a,72
EX 130
MA 132,a,28,a,76
EX 131
MC 52,a,132,v,180,0
CV 180,128,129
CV 181,130,131
MA 52,a,140,v,192
RD a,64,236,0
RD a,68,236,512
MA 52,a,148,v,196
RD v,204,239,4096
RD v,208,240,4096
MA 52,a,156,v,200
RD v,212,241,4096
RD a,72,236,2048
MC 136,a,0,a,80,-4.0
s_waitcnt lgkmcnt(9)
EX 132
MA 136,a,4,a,84
s_waitcnt lgkmcnt(8)
EX 133
MA 136,a,8,a,88
s_waitcnt lgkmcnt(7)
EX 134
MA 136,a,12,a,92
EX 135
MC 140,a,16,a,80,-4.0
CV 182,132,133
CV 183,134,135
MA 140,a,20,a,84
WR 238,180,12288
MA 140,a,24,a,88
s_add_u32 m0, s65, 0x18800
MA 140,a,28,a,92
s_waitcnt vmcnt(7)
s_mov_b32 s53, 0x8800
buffer_load_dwordx4 v249, s[44:47], s53 offen offset:0 nt lds
MC 216,v,220,v,176,0
EX 136
MA 216,v,224,v,184
s_waitcnt lgkmcnt(2)
s_barrier
MC 56,a,128,v,188,0
EX 137
MA 56,a,136,v,204
EX 138
MA 56,a,144,v,208
EX 139
MA 56,a,152,v,212
RD a,76,236,2560
RD a,80,236,4096
MC 60,a,132,v,188,0
CV 188,136,137
CV 189,138,139
MA 60,a,140,v,204
RD v,192,239,8192
RD v,196,240,8192
v_mov_b32 v232, v216
MA 60,a,148,v,208
RD v,200,241,8192
RD a,84,236,4608
MA 60,a,156,v,212
RD a,88,236,6144
RD a,92,236,6656
v_lshlrev_b32 v252, 16, v144
v_lshlrev_b32 v254, 16, v145
v_and_b32 v253, 0xffff0000, v144
v_and_b32 v255, 0xffff0000, v145
v_pk_mul_f32 v[252:253], v[252:253], s[26:27]
v_pk_mul_f32 v[254:255], v[254:255], s[26:27]
CV 144,252,253
CV 145,254,255
v_accvgpr_write_b32 a32, v144
v_accvgpr_write_b32 a33, v145
v_lshlrev_b32 v252, 16, v146
v_lshlrev_b32 v254, 16, v147
v_and_b32 v253, 0xffff0000, v146
v_and_b32 v255, 0xffff0000, v147
v_pk_mul_f32 v[252:253], v[252:253], s[26:27]
v_pk_mul_f32 v[254:255], v[254:255], s[26:27]
CV 146,252,253
CV 147,254,255
v_accvgpr_write_b32 a34, v146
v_accvgpr_write_b32 a35, v147
v_lshlrev_b32 v252, 16, v148
v_lshlrev_b32 v254, 16, v149
v_and_b32 v253, 0xffff0000, v148
v_and_b32 v255, 0xffff0000, v149
v_pk_mul_f32 v[252:253], v[252:253], s[26:27]
v_pk_mul_f32 v[254:255], v[254:255], s[26:27]
CV 148,252,253
CV 149,254,255
v_accvgpr_write_b32 a36, v148
v_accvgpr_write_b32 a37, v149
v_lshlrev_b32 v252, 16, v150
v_lshlrev_b32 v254, 16, v151
v_and_b32 v253, 0xffff0000, v150
v_and_b32 v255, 0xffff0000, v151
v_pk_mul_f32 v[252:253], v[252:253], s[26:27]
v_pk_mul_f32 v[254:255], v[254:255], s[26:27]
CV 150,252,253
CV 151,254,255
v_accvgpr_write_b32 a38, v150
v_accvgpr_write_b32 a39, v151
v_lshlrev_b32 v252, 16, v152
v_lshlrev_b32 v254, 16, v153
v_and_b32 v253, 0xffff0000, v152
v_and_b32 v255, 0xffff0000, v153
v_pk_mul_f32 v[252:253], v[252:253], s[26:27]
v_pk_mul_f32 v[254:255], v[254:255], s[26:27]
CV 152,252,253
CV 153,254,255
v_accvgpr_write_b32 a40, v152
v_accvgpr_write_b32 a41, v153
v_lshlrev_b32 v252, 16, v154
v_lshlrev_b32 v254, 16, v155
v_and_b32 v253, 0xffff0000, v154
v_and_b32 v255, 0xffff0000, v155
v_pk_mul_f32 v[252:253], v[252:253], s[26:27]
v_pk_mul_f32 v[254:255], v[254:255], s[26:27]
CV 154,252,253
CV 155,254,255
v_accvgpr_write_b32 a42, v154
v_accvgpr_write_b32 a43, v155
s_waitcnt vmcnt(7)
v_lshlrev_b32 v252, 16, v156
v_lshlrev_b32 v254, 16, v157
v_and_b32 v253, 0xffff0000, v156
v_and_b32 v255, 0xffff0000, v157
v_pk_mul_f32 v[252:253], v[252:253], s[26:27]
v_pk_mul_f32 v[254:255], v[254:255], s[26:27]
CV 156,252,253
CV 157,254,255
v_accvgpr_write_b32 a44, v156
v_accvgpr_write_b32 a45, v157
v_lshlrev_b32 v252, 16, v158
v_lshlrev_b32 v254, 16, v159
v_and_b32 v253, 0xffff0000, v158
v_and_b32 v255, 0xffff0000, v159
v_pk_mul_f32 v[252:253], v[252:253], s[26:27]
v_pk_mul_f32 v[254:255], v[254:255], s[26:27]
CV 158,252,253
CV 159,254,255
v_accvgpr_write_b32 a46, v158
v_accvgpr_write_b32 a47, v159
s_add_u32 m0, s65, 0x18c00
s_mov_b32 s53, 0x8c00
buffer_load_dwordx4 v249, s[44:47], s53 offen offset:0 nt lds
s_add_u32 m0, s65, 0x19000
s_mov_b32 s53, 0x9000
s_waitcnt vmcnt(7)
buffer_load_dwordx4 v249, s[44:47], s53 offen offset:0 nt lds
s_add_u32 m0, s65, 0x19400
s_mov_b32 s53, 0x9400
s_waitcnt vmcnt(7)
buffer_load_dwordx4 v249, s[44:47], s53 offen offset:0 nt lds
s_add_u32 m0, s65, 0x19800
s_mov_b32 s53, 0x9800
s_waitcnt vmcnt(7)
buffer_load_dwordx4 v249, s[44:47], s53 offen offset:0 nt lds
v_lshlrev_b32 v252, 16, v160
v_lshlrev_b32 v254, 16, v161
v_and_b32 v253, 0xffff0000, v160
v_and_b32 v255, 0xffff0000, v161
v_pk_mul_f32 v[252:253], v[252:253], s[26:27]
v_pk_mul_f32 v[254:255], v[254:255], s[26:27]
CV 160,252,253
CV 161,254,255
v_accvgpr_write_b32 a48, v160
v_accvgpr_write_b32 a49, v161
v_lshlrev_b32 v252, 16, v162
v_lshlrev_b32 v254, 16, v163
v_and_b32 v253, 0xffff0000, v162
v_and_b32 v255, 0xffff0000, v163
v_pk_mul_f32 v[252:253], v[252:253], s[26:27]
v_pk_mul_f32 v[254:255], v[254:255], s[26:27]
CV 162,252,253
CV 163,254,255
v_accvgpr_write_b32 a50, v162
v_accvgpr_write_b32 a51, v163
v_lshlrev_b32 v252, 16, v164
v_lshlrev_b32 v254, 16, v165
v_and_b32 v253, 0xffff0000, v164
v_and_b32 v255, 0xffff0000, v165
v_pk_mul_f32 v[252:253], v[252:253], s[26:27]
v_pk_mul_f32 v[254:255], v[254:255], s[26:27]
CV 164,252,253
CV 165,254,255
v_accvgpr_write_b32 a52, v164
v_accvgpr_write_b32 a53, v165
v_lshlrev_b32 v252, 16, v166
v_lshlrev_b32 v254, 16, v167
v_and_b32 v253, 0xffff0000, v166
v_and_b32 v255, 0xffff0000, v167
v_pk_mul_f32 v[252:253], v[252:253], s[26:27]
v_pk_mul_f32 v[254:255], v[254:255], s[26:27]
CV 166,252,253
CV 167,254,255
v_accvgpr_write_b32 a54, v166
v_accvgpr_write_b32 a55, v167
v_lshlrev_b32 v252, 16, v168
v_lshlrev_b32 v254, 16, v169
v_and_b32 v253, 0xffff0000, v168
v_and_b32 v255, 0xffff0000, v169
v_pk_mul_f32 v[252:253], v[252:253], s[26:27]
v_pk_mul_f32 v[254:255], v[254:255], s[26:27]
CV 168,252,253
CV 169,254,255
v_accvgpr_write_b32 a56, v168
v_accvgpr_write_b32 a57, v169
v_lshlrev_b32 v252, 16, v170
v_lshlrev_b32 v254, 16, v171
v_and_b32 v253, 0xffff0000, v170
v_and_b32 v255, 0xffff0000, v171
v_pk_mul_f32 v[252:253], v[252:253], s[26:27]
v_pk_mul_f32 v[254:255], v[254:255], s[26:27]
CV 170,252,253
CV 171,254,255
v_accvgpr_write_b32 a58, v170
v_accvgpr_write_b32 a59, v171
s_waitcnt vmcnt(7)
v_lshlrev_b32 v252, 16, v172
v_lshlrev_b32 v254, 16, v173
v_and_b32 v253, 0xffff0000, v172
v_and_b32 v255, 0xffff0000, v173
v_pk_mul_f32 v[252:253], v[252:253], s[26:27]
v_pk_mul_f32 v[254:255], v[254:255], s[26:27]
CV 172,252,253
CV 173,254,255
v_accvgpr_write_b32 a60, v172
v_accvgpr_write_b32 a61, v173
v_lshlrev_b32 v252, 16, v174
v_lshlrev_b32 v254, 16, v175
v_and_b32 v253, 0xffff0000, v174
v_and_b32 v255, 0xffff0000, v175
v_pk_mul_f32 v[252:253], v[252:253], s[26:27]
v_pk_mul_f32 v[254:255], v[254:255], s[26:27]
CV 174,252,253
CV 175,254,255
v_accvgpr_write_b32 a62, v174
v_accvgpr_write_b32 a63, v175
MC 128,a,32,a,64,-4.0
EX 140
MA 128,a,36,a,68
s_waitcnt lgkmcnt(9)
)"
R"(EX 141
MA 128,a,40,a,72
s_waitcnt lgkmcnt(7)
EX 142
MA 128,a,44,a,76
EX 143
MC 64,a,128,v,176,0
s_waitcnt lgkmcnt(4)
CV 190,140,141
CV 191,142,143
MA 64,a,136,v,192
WR 238,188,4096
MA 64,a,144,v,196
s_waitcnt lgkmcnt(4)
s_add_u32 m0, s65, 0x19c00
MA 64,a,152,v,200
s_mov_b32 s53, 0x9c00
buffer_load_dwordx4 v249, s[44:47], s53 offen offset:0 nt lds
MC 132,a,48,a,64,-4.0
EX 128
MA 132,a,52,a,68
EX 129
MA 132,a,56,a,72
EX 130
MA 132,a,60,a,76
EX 131
MC 68,a,132,v,176,0
CV 176,128,129
CV 177,130,131
MA 68,a,140,v,192
RD a,64,236,8192
RD a,68,236,8704
MA 68,a,148,v,196
RD v,204,239,0
RD v,208,240,0
MA 68,a,156,v,200
RD v,212,241,0
RD a,72,236,10240
MC 136,a,32,a,80,-4.0
s_waitcnt lgkmcnt(9)
EX 132
MA 136,a,36,a,84
s_waitcnt lgkmcnt(8)
EX 133
MA 136,a,40,a,88
s_waitcnt lgkmcnt(7)
EX 134
MA 136,a,44,a,92
EX 135
MC 140,a,48,a,80,-4.0
CV 178,132,133
CV 179,134,135
MA 140,a,52,a,84
WR 238,176,16384
MA 140,a,56,a,88
s_mov_b32 s53, 0x10000
MA 140,a,60,a,92
s_waitcnt vmcnt(7)
buffer_load_dwordx4 v[96:99], v248, s[40:43], s53 offen offset:0 nt
MC 216,v,220,v,180,0
EX 136
MA 216,v,224,v,188
s_waitcnt lgkmcnt(2)
s_barrier
MC 72,a,128,v,184,0
EX 137
MA 72,a,136,v,204
EX 138
MA 72,a,144,v,208
EX 139
MA 72,a,152,v,212
RD a,76,236,10752
RD a,80,236,12288
MC 76,a,132,v,184,0
CV 184,136,137
CV 185,138,139
MA 76,a,140,v,204
RD v,192,239,12288
RD v,196,240,12288
v_mov_b32 v233, v216
MA 76,a,148,v,208
RD v,200,241,12288
RD a,84,236,12800
MA 76,a,156,v,212
RD a,88,236,14336
RD a,92,236,14848
CP7_%=:
s_add_u32 s53, s24, CP7_%=-PCREF%=
s_lshr_b32 s55, s65, 3
s_add_u32 m0, s55, 0x27000
s_nop 0
buffer_load_dwordx4 v238, s[20:23], s53 offen lds
MC 128,a,32,a,64,-4.0
EX 140
MA 128,a,36,a,68
s_waitcnt lgkmcnt(9)
EX 141
MA 128,a,40,a,72
s_waitcnt lgkmcnt(7)
EX 142
MA 128,a,44,a,76
EX 143
MC 80,a,128,v,180,0
s_waitcnt lgkmcnt(4)
CV 186,140,141
CV 187,142,143
MA 80,a,136,v,192
WR 238,184,0
MA 80,a,144,v,196
s_waitcnt lgkmcnt(4)
s_mov_b32 s53, 0x10000
MA 80,a,152,v,200
s_waitcnt vmcnt(8)
buffer_load_dwordx4 v[100:103], v248, s[40:43], s53 offen offset:64 nt
MC 132,a,48,a,64,-4.0
EX 128
MA 132,a,52,a,68
EX 129
MA 132,a,56,a,72
EX 130
MA 132,a,60,a,76
EX 131
MC 84,a,132,v,180,0
CV 180,128,129
CV 181,130,131
MA 84,a,140,v,192
RD a,64,236,16384
RD a,68,236,16896
MA 84,a,148,v,196
RD v,204,239,4096
RD v,208,240,4096
MA 84,a,156,v,200
RD v,212,241,4096
RD a,72,236,18432
MC 136,a,32,a,80,-4.0
s_waitcnt lgkmcnt(9)
EX 132
MA 136,a,36,a,84
s_waitcnt lgkmcnt(8)
EX 133
MA 136,a,40,a,88
s_waitcnt lgkmcnt(7)
EX 134
MA 136,a,44,a,92
EX 135
MC 140,a,48,a,80,-4.0
CV 182,132,133
CV 183,134,135
MA 140,a,52,a,84
WR 238,180,20480
MA 140,a,56,a,88
s_mov_b32 s53, 0x10000
MA 140,a,60,a,92
s_waitcnt vmcnt(8)
buffer_load_dwordx4 v[104:107], v248, s[40:43], s53 offen offset:128 nt
MC 216,v,220,v,176,0
EX 136
MA 216,v,224,v,184
s_waitcnt lgkmcnt(2)
s_barrier
MC 88,a,128,v,188,0
EX 137
MA 88,a,136,v,204
EX 138
MA 88,a,144,v,208
EX 139
MA 88,a,152,v,212
RD a,76,236,18944
RD a,80,236,20480
MC 92,a,132,v,188,0
CV 188,136,137
CV 189,138,139
MA 92,a,140,v,204
RD v,192,239,16384
RD v,196,240,16384
v_add_f32 v228, v228, v216
MA 92,a,148,v,208
RD v,200,241,16384
RD a,84,236,20992
MA 92,a,156,v,212
RD a,88,236,22528
RD a,92,236,23040
s_mov_b32 s53, 0x10000
s_waitcnt vmcnt(8)
buffer_load_dwordx4 v[108:111], v248, s[40:43], s53 offen offset:192 nt
s_mov_b32 s53, 0x11000
s_waitcnt vmcnt(8)
buffer_load_dwordx4 v[112:115], v248, s[40:43], s53 offen offset:0 nt
s_mov_b32 s53, 0x11000
s_waitcnt vmcnt(8)
buffer_load_dwordx4 v[116:119], v248, s[40:43], s53 offen offset:64 nt
s_mov_b32 s53, 0x11000
s_waitcnt vmcnt(8)
buffer_load_dwordx4 v[120:123], v248, s[40:43], s53 offen offset:128 nt
s_waitcnt vmcnt(8)
s_barrier
v_add_u32 v252, s56, v250
v_add_u32 v253, s57, v250
v_add_u32 v254, s58, v250
v_add_u32 v255, s59, v250
ds_read_b64_tr_b16 a[160:161], v252 offset:32768
ds_read_b64_tr_b16 a[162:163], v252 offset:36864
ds_read_b64_tr_b16 a[168:169], v253 offset:32768
ds_read_b64_tr_b16 a[170:171], v253 offset:36864
ds_read_b64_tr_b16 a[176:177], v254 offset:32768
s_waitcnt lgkmcnt(14)
ds_read_b64_tr_b16 a[178:179], v254 offset:36864
s_waitcnt lgkmcnt(14)
ds_read_b64_tr_b16 a[184:185], v255 offset:32768
s_waitcnt lgkmcnt(14)
ds_read_b64_tr_b16 a[186:187], v255 offset:36864
s_waitcnt lgkmcnt(14)
ds_read_b64_tr_b16 a[164:165], v252 offset:32776
s_waitcnt lgkmcnt(14)
ds_read_b64_tr_b16 a[166:167], v252 offset:36872
s_waitcnt lgkmcnt(14)
ds_read_b64_tr_b16 a[172:173], v253 offset:32776
s_waitcnt lgkmcnt(14)
ds_read_b64_tr_b16 a[174:175], v253 offset:36872
s_waitcnt lgkmcnt(14)
ds_read_b64_tr_b16 a[180:181], v254 offset:32776
s_waitcnt lgkmcnt(14)
ds_read_b64_tr_b16 a[182:183], v254 offset:36872
s_waitcnt lgkmcnt(14)
ds_read_b64_tr_b16 a[188:189], v255 offset:32776
s_waitcnt lgkmcnt(14)
ds_read_b64_tr_b16 a[190:191], v255 offset:36872
MC 128,a,32,a,64,-4.0
EX 140
MA 128,a,36,a,68
EX 141
MA 128,a,40,a,72
EX 142
MA 128,a,44,a,76
s_waitcnt lgkmcnt(14)
EX 143
MA 0,a,160,v,176
s_waitcnt lgkmcnt(12)
CV 190,140,141
CV 191,142,143
MA 0,a,168,v,192
s_waitcnt lgkmcnt(10)
WR 238,188,4096
MA 0,a,176,v,196
s_waitcnt lgkmcnt(9)
s_mov_b32 s53, 0x11000
MA 0,a,184,v,200
buffer_load_dwordx4 v[124:127], v248, s[40:43], s53 offen offset:192 nt
MC 132,a,48,a,64,-4.0
EX 128
MA 132,a,52,a,68
EX 129
MA 132,a,56,a,72
EX 130
MA 132,a,60,a,76
s_waitcnt lgkmcnt(7)
EX 131
MA 4,a,164,v,176
s_waitcnt lgkmcnt(5)
CV 176,128,129
CV 177,130,131
MA 4,a,172,v,192
s_waitcnt lgkmcnt(3)
RD a,64,236,24576
RD a,68,236,25088
MA 4,a,180,v,196
s_waitcnt lgkmcnt(3)
RD v,204,239,0
RD v,208,240,0
MA 4,a,188,v,200
RD v,212,241,0
RD a,72,236,26624
MC 136,a,32,a,80,-4.0
EX 132
MA 136,a,36,a,84
EX 133
MA 136,a,40,a,88
EX 134
MA 136,a,44,a,92
EX 135
MC 140,a,48,a,80,-4.0
CV 178,132,133
CV 179,134,135
MA 140,a,52,a,84
WR 238,176,8192
MA 140,a,56,a,88
s_add_u32 m0, s65, 0x10000
MA 140,a,60,a,92
s_waitcnt vmcnt(8)
s_mov_b32 s53, 0x10000
buffer_load_dwordx4 v249, s[44:47], s53 offen offset:0 nt lds
MC 216,v,220,v,180,0
EX 136
MA 216,v,224,v,188
s_waitcnt lgkmcnt(2)
s_barrier
MA 8,a,160,v,184
EX 137
MA 8,a,168,v,204
EX 138
MA 8,a,176,v,208
EX 139
MA 8,a,184,v,212
RD a,76,236,27136
RD a,80,236,28672
MA 12,a,164,v,184
CV 184,136,137
CV 185,138,139
MA 12,a,172,v,204
RD v,192,239,20480
RD v,196,240,20480
v_add_f32 v229, v229, v216
MA 12,a,180,v,208
RD v,200,241,20480
RD a,84,236,29184
MA 12,a,188,v,212
RD a,88,236,30720
RD a,92,236,31232
MC 128,a,32,a,64,-4.0
EX 140
MA 128,a,36,a,68
s_waitcnt lgkmcnt(9)
EX 141
MA 128,a,40,a,72
s_waitcnt lgkmcnt(7)
EX 142
MA 128,a,44,a,76
EX 143
MA 16,a,160,v,180
s_waitcnt lgkmcnt(4)
CV 186,140,141
CV 187,142,143
MA 16,a,168,v,192
WR 238,184,0
MA 16,a,176,v,196
s_waitcnt lgkmcnt(4)
s_add_u32 m0, s65, 0x10400
MA 16,a,184,v,200
s_waitcnt vmcnt(7)
s_mov_b32 s53, 0x10400
buffer_load_dwordx4 v249, s[44:47], s53 offen offset:0 nt lds
MC 132,a,48,a,64,-4.0
EX 128
MA 132,a,52,a,68
EX 129
MA 132,a,56,a,72
EX 130
MA 132,a,60,a,76
EX 131
MA 20,a,164,v,180
CV 180,128,129
CV 181,130,131
MA 20,a,172,v,192
RD a,64,236,32768
RD a,68,236,33280
MA 20,a,180,v,196
RD v,204,239,4096
RD v,208,240,4096
MA 20,a,188,v,200
RD v,212,241,4096
RD a,72,236,34816
MC 136,a,32,a,80,-4.0
s_waitcnt lgkmcnt(9)
EX 132
MA 136,a,36,a,84
s_waitcnt lgkmcnt(8)
EX 133
MA 136,a,40,a,88
s_waitcnt lgkmcnt(7)
EX 134
MA 136,a,44,a,92
EX 135
MC 140,a,48,a,80,-4.0
CV 182,132,133
CV 183,134,135
MA 140,a,52,a,84
WR 238,180,12288
MA 140,a,56,a,88
s_add_u32 m0, s65, 0x10800
MA 140,a,60,a,92
s_waitcnt vmcnt(7)
s_mov_b32 s53, 0x10800
buffer_load_dwordx4 v249, s[44:47], s53 offen offset:0 nt lds
MC 216,v,220,v,176,0
EX 136
MA 216,v,224,v,184
s_waitcnt lgkmcnt(2)
s_barrier
MA 24,a,160,v,188
EX 137
MA 24,a,168,v,204
EX 138
MA 24,a,176,v,208
EX 139
MA 24,a,184,v,212
RD a,76,236,35328
RD a,80,236,36864
MA 28,a,164,v,188
CV 188,136,137
CV 189,138,139
)"
R"(MA 28,a,172,v,204
RD v,192,239,8192
RD v,196,240,8192
v_add_f32 v230, v230, v216
MA 28,a,180,v,208
RD v,200,241,8192
RD a,84,236,37376
MA 28,a,188,v,212
RD a,88,236,38912
RD a,92,236,39424
MC 128,a,32,a,64,-4.0
EX 140
MA 128,a,36,a,68
s_waitcnt lgkmcnt(9)
EX 141
MA 128,a,40,a,72
s_waitcnt lgkmcnt(7)
EX 142
MA 128,a,44,a,76
EX 143
MA 32,a,160,v,176
s_waitcnt lgkmcnt(4)
CV 190,140,141
CV 191,142,143
MA 32,a,168,v,192
WR 238,188,4096
MA 32,a,176,v,196
s_waitcnt lgkmcnt(4)
s_add_u32 m0, s65, 0x10c00
MA 32,a,184,v,200
s_waitcnt vmcnt(7)
s_mov_b32 s53, 0x10c00
buffer_load_dwordx4 v249, s[44:47], s53 offen offset:0 nt lds
MC 132,a,48,a,64,-4.0
EX 128
MA 132,a,52,a,68
EX 129
MA 132,a,56,a,72
EX 130
MA 132,a,60,a,76
EX 131
MA 36,a,164,v,176
CV 176,128,129
CV 177,130,131
MA 36,a,172,v,192
RD a,64,236,40960
RD a,68,236,41472
MA 36,a,180,v,196
RD v,204,239,0
RD v,208,240,0
MA 36,a,188,v,200
RD v,212,241,0
RD a,72,236,43008
MC 136,a,32,a,80,-4.0
s_waitcnt lgkmcnt(9)
EX 132
MA 136,a,36,a,84
s_waitcnt lgkmcnt(8)
EX 133
MA 136,a,40,a,88
s_waitcnt lgkmcnt(7)
EX 134
MA 136,a,44,a,92
EX 135
MC 140,a,48,a,80,-4.0
CV 178,132,133
CV 179,134,135
MA 140,a,52,a,84
WR 238,176,16384
MA 140,a,56,a,88
s_add_u32 m0, s65, 0x11000
MA 140,a,60,a,92
s_waitcnt vmcnt(7)
s_mov_b32 s53, 0x11000
buffer_load_dwordx4 v249, s[44:47], s53 offen offset:0 nt lds
MC 216,v,220,v,180,0
EX 136
MA 216,v,224,v,188
s_waitcnt lgkmcnt(2)
s_barrier
MA 40,a,160,v,184
EX 137
MA 40,a,168,v,204
EX 138
MA 40,a,176,v,208
EX 139
MA 40,a,184,v,212
RD a,76,236,43520
RD a,80,236,45056
MA 44,a,164,v,184
CV 184,136,137
CV 185,138,139
MA 44,a,172,v,204
RD v,192,239,12288
RD v,196,240,12288
v_add_f32 v231, v231, v216
MA 44,a,180,v,208
RD v,200,241,12288
RD a,84,236,45568
MA 44,a,188,v,212
RD a,88,236,47104
RD a,92,236,47616
MC 128,a,32,a,64,-4.0
EX 140
MA 128,a,36,a,68
s_waitcnt lgkmcnt(9)
EX 141
MA 128,a,40,a,72
s_waitcnt lgkmcnt(7)
EX 142
MA 128,a,44,a,76
EX 143
MA 48,a,160,v,180
s_waitcnt lgkmcnt(4)
CV 186,140,141
CV 187,142,143
MA 48,a,168,v,192
WR 238,184,0
MA 48,a,176,v,196
s_waitcnt lgkmcnt(4)
s_add_u32 m0, s65, 0x11400
MA 48,a,184,v,200
s_waitcnt vmcnt(7)
s_mov_b32 s53, 0x11400
buffer_load_dwordx4 v249, s[44:47], s53 offen offset:0 nt lds
MC 132,a,48,a,64,-4.0
EX 128
MA 132,a,52,a,68
EX 129
MA 132,a,56,a,72
EX 130
MA 132,a,60,a,76
EX 131
MA 52,a,164,v,180
CV 180,128,129
CV 181,130,131
MA 52,a,172,v,192
RD a,64,236,0
RD a,68,236,512
MA 52,a,180,v,196
RD v,204,239,4096
RD v,208,240,4096
MA 52,a,188,v,200
RD v,212,241,4096
RD a,72,236,2048
MC 136,a,32,a,80,-4.0
s_waitcnt lgkmcnt(9)
EX 132
MA 136,a,36,a,84
s_waitcnt lgkmcnt(8)
EX 133
MA 136,a,40,a,88
s_waitcnt lgkmcnt(7)
EX 134
MA 136,a,44,a,92
EX 135
MC 140,a,48,a,80,-4.0
CV 182,132,133
CV 183,134,135
MA 140,a,52,a,84
WR 238,180,20480
MA 140,a,56,a,88
s_add_u32 m0, s65, 0x11800
MA 140,a,60,a,92
s_waitcnt vmcnt(7)
s_mov_b32 s53, 0x11800
buffer_load_dwordx4 v249, s[44:47], s53 offen offset:0 nt lds
MC 216,v,220,v,176,0
EX 136
MA 216,v,224,v,184
s_waitcnt lgkmcnt(2)
s_barrier
MA 56,a,160,v,188
EX 137
MA 56,a,168,v,204
EX 138
MA 56,a,176,v,208
EX 139
MA 56,a,184,v,212
RD a,76,236,2560
RD a,80,236,4096
MA 60,a,164,v,188
CV 188,136,137
CV 189,138,139
MA 60,a,172,v,204
RD v,192,239,16384
RD v,196,240,16384
v_add_f32 v232, v232, v216
MA 60,a,180,v,208
RD v,200,241,16384
RD a,84,236,4608
MA 60,a,188,v,212
RD a,88,236,6144
RD a,92,236,6656
v_lshlrev_b32 v252, 16, v96
v_lshlrev_b32 v254, 16, v97
v_and_b32 v253, 0xffff0000, v96
v_and_b32 v255, 0xffff0000, v97
v_pk_mul_f32 v[252:253], v[252:253], s[26:27]
v_pk_mul_f32 v[254:255], v[254:255], s[26:27]
CV 96,252,253
CV 97,254,255
v_lshlrev_b32 v252, 16, v98
v_lshlrev_b32 v254, 16, v99
v_and_b32 v253, 0xffff0000, v98
v_and_b32 v255, 0xffff0000, v99
v_pk_mul_f32 v[252:253], v[252:253], s[26:27]
v_pk_mul_f32 v[254:255], v[254:255], s[26:27]
CV 98,252,253
CV 99,254,255
v_lshlrev_b32 v252, 16, v100
v_lshlrev_b32 v254, 16, v101
v_and_b32 v253, 0xffff0000, v100
v_and_b32 v255, 0xffff0000, v101
v_pk_mul_f32 v[252:253], v[252:253], s[26:27]
v_pk_mul_f32 v[254:255], v[254:255], s[26:27]
CV 100,252,253
CV 101,254,255
v_lshlrev_b32 v252, 16, v102
v_lshlrev_b32 v254, 16, v103
v_and_b32 v253, 0xffff0000, v102
v_and_b32 v255, 0xffff0000, v103
v_pk_mul_f32 v[252:253], v[252:253], s[26:27]
v_pk_mul_f32 v[254:255], v[254:255], s[26:27]
CV 102,252,253
CV 103,254,255
v_lshlrev_b32 v252, 16, v104
v_lshlrev_b32 v254, 16, v105
v_and_b32 v253, 0xffff0000, v104
v_and_b32 v255, 0xffff0000, v105
v_pk_mul_f32 v[252:253], v[252:253], s[26:27]
v_pk_mul_f32 v[254:255], v[254:255], s[26:27]
CV 104,252,253
CV 105,254,255
v_lshlrev_b32 v252, 16, v106
v_lshlrev_b32 v254, 16, v107
v_and_b32 v253, 0xffff0000, v106
v_and_b32 v255, 0xffff0000, v107
v_pk_mul_f32 v[252:253], v[252:253], s[26:27]
v_pk_mul_f32 v[254:255], v[254:255], s[26:27]
CV 106,252,253
CV 107,254,255
v_lshlrev_b32 v252, 16, v108
v_lshlrev_b32 v254, 16, v109
v_and_b32 v253, 0xffff0000, v108
v_and_b32 v255, 0xffff0000, v109
v_pk_mul_f32 v[252:253], v[252:253], s[26:27]
v_pk_mul_f32 v[254:255], v[254:255], s[26:27]
CV 108,252,253
CV 109,254,255
v_lshlrev_b32 v252, 16, v110
v_lshlrev_b32 v254, 16, v111
v_and_b32 v253, 0xffff0000, v110
v_and_b32 v255, 0xffff0000, v111
v_pk_mul_f32 v[252:253], v[252:253], s[26:27]
v_pk_mul_f32 v[254:255], v[254:255], s[26:27]
CV 110,252,253
CV 111,254,255
v_lshlrev_b32 v252, 16, v112
v_lshlrev_b32 v254, 16, v113
v_and_b32 v253, 0xffff0000, v112
v_and_b32 v255, 0xffff0000, v113
v_pk_mul_f32 v[252:253], v[252:253], s[26:27]
v_pk_mul_f32 v[254:255], v[254:255], s[26:27]
CV 112,252,253
CV 113,254,255
v_lshlrev_b32 v252, 16, v114
v_lshlrev_b32 v254, 16, v115
v_and_b32 v253, 0xffff0000, v114
v_and_b32 v255, 0xffff0000, v115
v_pk_mul_f32 v[252:253], v[252:253], s[26:27]
v_pk_mul_f32 v[254:255], v[254:255], s[26:27]
CV 114,252,253
CV 115,254,255
v_lshlrev_b32 v252, 16, v116
v_lshlrev_b32 v254, 16, v117
v_and_b32 v253, 0xffff0000, v116
v_and_b32 v255, 0xffff0000, v117
v_pk_mul_f32 v[252:253], v[252:253], s[26:27]
v_pk_mul_f32 v[254:255], v[254:255], s[26:27]
CV 116,252,253
CV 117,254,255
v_lshlrev_b32 v252, 16, v118
v_lshlrev_b32 v254, 16, v119
v_and_b32 v253, 0xffff0000, v118
v_and_b32 v255, 0xffff0000, v119
v_pk_mul_f32 v[252:253], v[252:253], s[26:27]
v_pk_mul_f32 v[254:255], v[254:255], s[26:27]
CV 118,252,253
CV 119,254,255
v_lshlrev_b32 v252, 16, v120
v_lshlrev_b32 v254, 16, v121
v_and_b32 v253, 0xffff0000, v120
v_and_b32 v255, 0xffff0000, v121
v_pk_mul_f32 v[252:253], v[252:253], s[26:27]
v_pk_mul_f32 v[254:255], v[254:255], s[26:27]
CV 120,252,253
CV 121,254,255
v_lshlrev_b32 v252, 16, v122
v_lshlrev_b32 v254, 16, v123
v_and_b32 v253, 0xffff0000, v122
v_and_b32 v255, 0xffff0000, v123
v_pk_mul_f32 v[252:253], v[252:253], s[26:27]
v_pk_mul_f32 v[254:255], v[254:255], s[26:27]
CV 122,252,253
CV 123,254,255
s_waitcnt vmcnt(7)
v_lshlrev_b32 v252, 16, v124
v_lshlrev_b32 v254, 16, v125
v_and_b32 v253, 0xffff0000, v124
v_and_b32 v255, 0xffff0000, v125
v_pk_mul_f32 v[252:253], v[252:253], s[26:27]
v_pk_mul_f32 v[254:255], v[254:255], s[26:27]
CV 124,252,253
CV 125,254,255
v_lshlrev_b32 v252, 16, v126
v_lshlrev_b32 v254, 16, v127
v_and_b32 v253, 0xffff0000, v126
v_and_b32 v255, 0xffff0000, v127
v_pk_mul_f32 v[252:253], v[252:253], s[26:27]
v_pk_mul_f32 v[254:255], v[254:255], s[26:27]
CV 126,252,253
CV 127,254,255
MC 128,v,96,a,64,-4.0
EX 140
MA 128,v,100,a,68
s_waitcnt lgkmcnt(9)
EX 141
MA 128,v,104,a,72
s_waitcnt lgkmcnt(7)
EX 142
MA 128,v,108,a,76
EX 143
MA 64,a,160,v,176
s_waitcnt lgkmcnt(4)
CV 190,140,141
CV 191,142,143
MA 64,a,168,v,192
WR 238,188,4096
MA 64,a,176,v,196
s_waitcnt lgkmcnt(4)
s_add_u32 m0, s65, 0x11c00
MA 64,a,184,v,200
s_mov_b32 s53, 0x11c00
buffer_load_dwordx4 v249, s[44:47], s53 offen offset:0 nt lds
MC 132,v,112,a,64,-4.0
EX 128
MA 132,v,116,a,68
EX 129
MA 132,v,120,a,72
EX 130
MA 132,v,124,a,76
EX 131
MA 68,a,164,v,176
CV 176,128,129
CV 177,130,131
MA 68,a,172,v,192
RD a,64,236,8192
RD a,68,236,8704
MA 68,a,180,v,196
RD v,204,239,0
RD v,208,240,0
MA 68,a,188,v,200
RD v,212,241,0
RD a,72,236,10240
MC 136,v,96,a,80,-4.0
s_waitcnt lgkmcnt(9)
EX 132
MA 136,v,100,a,84
s_waitcnt lgkmcnt(8)
EX 133
MA 136,v,104,a,88
s_waitcnt lgkmcnt(7)
EX 134
MA 136,v,108,a,92
EX 135
MC 140,v,112,a,80,-4.0
CV 178,132,133
CV 179,134,135
MA 140,v,116,a,84
WR 238,176,8192
MA 140,v,120,a,88
s_mov_b32 s53, 0x18000
MA 140,v,124,a,92
s_waitcnt vmcnt(7)
buffer_load_dwordx4 v[144:147], v248, s[40:43], s53 offen offset:0 nt
MC 216,v,220,v,180,0
EX 136
MA 216,v,224,v,188
s_waitcnt lgkmcnt(2)
s_barrier
MA 72,a,160,v,184
EX 137
MA 72,a,168,v,204
EX 138
MA 72,a,176,v,208
EX 139
MA 72,a,184,v,212
RD a,76,236,10752
RD a,80,236,12288
)"
R"(MA 76,a,164,v,184
CV 184,136,137
CV 185,138,139
MA 76,a,172,v,204
RD v,192,239,20480
RD v,196,240,20480
v_add_f32 v233, v233, v216
MA 76,a,180,v,208
RD v,200,241,20480
RD a,84,236,12800
MA 76,a,188,v,212
RD a,88,236,14336
RD a,92,236,14848
CP13_%=:
s_add_u32 s53, s24, CP13_%=-PCREF%=
s_lshr_b32 s55, s65, 3
s_add_u32 m0, s55, 0x27000
s_nop 0
buffer_load_dwordx4 v238, s[20:23], s53 offen lds
MC 128,v,96,a,64,-4.0
EX 140
MA 128,v,100,a,68
s_waitcnt lgkmcnt(9)
EX 141
MA 128,v,104,a,72
s_waitcnt lgkmcnt(7)
EX 142
MA 128,v,108,a,76
EX 143
MA 80,a,160,v,180
s_waitcnt lgkmcnt(4)
CV 186,140,141
CV 187,142,143
MA 80,a,168,v,192
WR 238,184,0
MA 80,a,176,v,196
s_waitcnt lgkmcnt(4)
s_mov_b32 s53, 0x18000
MA 80,a,184,v,200
s_waitcnt vmcnt(8)
buffer_load_dwordx4 v[148:151], v248, s[40:43], s53 offen offset:64 nt
MC 132,v,112,a,64,-4.0
EX 128
MA 132,v,116,a,68
EX 129
MA 132,v,120,a,72
EX 130
MA 132,v,124,a,76
EX 131
MA 84,a,164,v,180
CV 180,128,129
CV 181,130,131
MA 84,a,172,v,192
RD a,64,236,16384
RD a,68,236,16896
MA 84,a,180,v,196
RD v,204,239,4096
RD v,208,240,4096
MA 84,a,188,v,200
RD v,212,241,4096
RD a,72,236,18432
MC 136,v,96,a,80,-4.0
s_waitcnt lgkmcnt(9)
EX 132
MA 136,v,100,a,84
s_waitcnt lgkmcnt(8)
EX 133
MA 136,v,104,a,88
s_waitcnt lgkmcnt(7)
EX 134
MA 136,v,108,a,92
EX 135
MC 140,v,112,a,80,-4.0
CV 182,132,133
CV 183,134,135
MA 140,v,116,a,84
WR 238,180,12288
MA 140,v,120,a,88
s_mov_b32 s53, 0x18000
MA 140,v,124,a,92
s_waitcnt vmcnt(8)
buffer_load_dwordx4 v[152:155], v248, s[40:43], s53 offen offset:128 nt
MC 216,v,220,v,176,0
EX 136
MA 216,v,224,v,184
s_waitcnt lgkmcnt(2)
s_barrier
MA 88,a,160,v,188
EX 137
MA 88,a,168,v,204
EX 138
MA 88,a,176,v,208
EX 139
MA 88,a,184,v,212
RD a,76,236,18944
RD a,80,236,20480
MA 92,a,164,v,188
CV 188,136,137
CV 189,138,139
MA 92,a,172,v,204
RD v,192,239,8192
RD v,196,240,8192
v_add_f32 v228, v228, v216
MA 92,a,180,v,208
RD v,200,241,8192
RD a,84,236,20992
MA 92,a,188,v,212
RD a,88,236,22528
RD a,92,236,23040
s_mov_b32 s53, 0x18000
s_waitcnt vmcnt(8)
buffer_load_dwordx4 v[156:159], v248, s[40:43], s53 offen offset:192 nt
s_mov_b32 s53, 0x19000
s_waitcnt vmcnt(8)
buffer_load_dwordx4 v[160:163], v248, s[40:43], s53 offen offset:0 nt
s_mov_b32 s53, 0x19000
s_waitcnt vmcnt(8)
buffer_load_dwordx4 v[164:167], v248, s[40:43], s53 offen offset:64 nt
s_mov_b32 s53, 0x19000
s_waitcnt vmcnt(8)
buffer_load_dwordx4 v[168:171], v248, s[40:43], s53 offen offset:128 nt
s_waitcnt vmcnt(8)
s_barrier
v_add_u32 v252, s56, v250
v_add_u32 v253, s57, v250
v_add_u32 v254, s58, v250
v_add_u32 v255, s59, v250
ds_read_b64_tr_b16 a[192:193], v252 offset:0
ds_read_b64_tr_b16 a[194:195], v252 offset:4096
ds_read_b64_tr_b16 a[200:201], v253 offset:0
ds_read_b64_tr_b16 a[202:203], v253 offset:4096
ds_read_b64_tr_b16 a[208:209], v254 offset:0
s_waitcnt lgkmcnt(14)
ds_read_b64_tr_b16 a[210:211], v254 offset:4096
s_waitcnt lgkmcnt(14)
ds_read_b64_tr_b16 a[216:217], v255 offset:0
s_waitcnt lgkmcnt(14)
ds_read_b64_tr_b16 a[218:219], v255 offset:4096
s_waitcnt lgkmcnt(14)
ds_read_b64_tr_b16 a[196:197], v252 offset:8
s_waitcnt lgkmcnt(14)
ds_read_b64_tr_b16 a[198:199], v252 offset:4104
s_waitcnt lgkmcnt(14)
ds_read_b64_tr_b16 a[204:205], v253 offset:8
s_waitcnt lgkmcnt(14)
ds_read_b64_tr_b16 a[206:207], v253 offset:4104
s_waitcnt lgkmcnt(14)
ds_read_b64_tr_b16 a[212:213], v254 offset:8
s_waitcnt lgkmcnt(14)
ds_read_b64_tr_b16 a[214:215], v254 offset:4104
s_waitcnt lgkmcnt(14)
ds_read_b64_tr_b16 a[220:221], v255 offset:8
s_waitcnt lgkmcnt(14)
ds_read_b64_tr_b16 a[222:223], v255 offset:4104
MC 128,v,96,a,64,-4.0
EX 140
MA 128,v,100,a,68
EX 141
MA 128,v,104,a,72
EX 142
MA 128,v,108,a,76
s_waitcnt lgkmcnt(14)
EX 143
MA 0,a,192,v,176
s_waitcnt lgkmcnt(12)
CV 190,140,141
CV 191,142,143
MA 0,a,200,v,192
s_waitcnt lgkmcnt(10)
WR 238,188,4096
MA 0,a,208,v,196
s_waitcnt lgkmcnt(9)
s_mov_b32 s53, 0x19000
MA 0,a,216,v,200
buffer_load_dwordx4 v[172:175], v248, s[40:43], s53 offen offset:192 nt
MC 132,v,112,a,64,-4.0
EX 128
MA 132,v,116,a,68
EX 129
MA 132,v,120,a,72
EX 130
MA 132,v,124,a,76
s_waitcnt lgkmcnt(7)
EX 131
MA 4,a,196,v,176
s_waitcnt lgkmcnt(5)
CV 176,128,129
CV 177,130,131
MA 4,a,204,v,192
s_waitcnt lgkmcnt(3)
RD a,64,236,24576
RD a,68,236,25088
MA 4,a,212,v,196
s_waitcnt lgkmcnt(3)
RD v,204,239,0
RD v,208,240,0
MA 4,a,220,v,200
RD v,212,241,0
RD a,72,236,26624
MC 136,v,96,a,80,-4.0
EX 132
MA 136,v,100,a,84
EX 133
MA 136,v,104,a,88
EX 134
MA 136,v,108,a,92
EX 135
MC 140,v,112,a,80,-4.0
CV 178,132,133
CV 179,134,135
MA 140,v,116,a,84
WR 238,176,16384
MA 140,v,120,a,88
s_add_u32 m0, s65, 0x18000
MA 140,v,124,a,92
s_waitcnt vmcnt(8)
s_mov_b32 s53, 0x18000
buffer_load_dwordx4 v249, s[44:47], s53 offen offset:0 nt lds
MC 216,v,220,v,180,0
EX 136
MA 216,v,224,v,188
s_waitcnt lgkmcnt(2)
s_barrier
MA 8,a,192,v,184
EX 137
MA 8,a,200,v,204
EX 138
MA 8,a,208,v,208
EX 139
MA 8,a,216,v,212
RD a,76,236,27136
RD a,80,236,28672
MA 12,a,196,v,184
CV 184,136,137
CV 185,138,139
MA 12,a,204,v,204
RD v,192,239,12288
RD v,196,240,12288
v_add_f32 v229, v229, v216
MA 12,a,212,v,208
RD v,200,241,12288
RD a,84,236,29184
MA 12,a,220,v,212
RD a,88,236,30720
RD a,92,236,31232
MC 128,v,96,a,64,-4.0
EX 140
MA 128,v,100,a,68
s_waitcnt lgkmcnt(9)
EX 141
MA 128,v,104,a,72
s_waitcnt lgkmcnt(7)
EX 142
MA 128,v,108,a,76
EX 143
MA 16,a,192,v,180
s_waitcnt lgkmcnt(4)
CV 186,140,141
CV 187,142,143
MA 16,a,200,v,192
WR 238,184,0
MA 16,a,208,v,196
s_waitcnt lgkmcnt(4)
s_add_u32 m0, s65, 0x18400
MA 16,a,216,v,200
s_waitcnt vmcnt(7)
s_mov_b32 s53, 0x18400
buffer_load_dwordx4 v249, s[44:47], s53 offen offset:0 nt lds
MC 132,v,112,a,64,-4.0
EX 128
MA 132,v,116,a,68
EX 129
MA 132,v,120,a,72
EX 130
MA 132,v,124,a,76
EX 131
MA 20,a,196,v,180
CV 180,128,129
CV 181,130,131
MA 20,a,204,v,192
RD a,64,236,32768
RD a,68,236,33280
MA 20,a,212,v,196
RD v,204,239,4096
RD v,208,240,4096
MA 20,a,220,v,200
RD v,212,241,4096
RD a,72,236,34816
MC 136,v,96,a,80,-4.0
s_waitcnt lgkmcnt(9)
EX 132
MA 136,v,100,a,84
s_waitcnt lgkmcnt(8)
EX 133
MA 136,v,104,a,88
s_waitcnt lgkmcnt(7)
EX 134
MA 136,v,108,a,92
EX 135
MC 140,v,112,a,80,-4.0
CV 182,132,133
CV 183,134,135
MA 140,v,116,a,84
WR 238,180,20480
MA 140,v,120,a,88
s_add_u32 m0, s65, 0x18800
MA 140,v,124,a,92
s_waitcnt vmcnt(7)
s_mov_b32 s53, 0x18800
buffer_load_dwordx4 v249, s[44:47], s53 offen offset:0 nt lds
MC 216,v,220,v,176,0
EX 136
MA 216,v,224,v,184
s_waitcnt lgkmcnt(2)
s_barrier
MA 24,a,192,v,188
EX 137
MA 24,a,200,v,204
EX 138
MA 24,a,208,v,208
EX 139
MA 24,a,216,v,212
RD a,76,236,35328
RD a,80,236,36864
MA 28,a,196,v,188
CV 188,136,137
CV 189,138,139
MA 28,a,204,v,204
RD v,192,239,16384
RD v,196,240,16384
v_add_f32 v230, v230, v216
MA 28,a,212,v,208
RD v,200,241,16384
RD a,84,236,37376
MA 28,a,220,v,212
RD a,88,236,38912
RD a,92,236,39424
MC 128,v,96,a,64,-4.0
EX 140
MA 128,v,100,a,68
s_waitcnt lgkmcnt(9)
EX 141
MA 128,v,104,a,72
s_waitcnt lgkmcnt(7)
EX 142
MA 128,v,108,a,76
EX 143
MA 32,a,192,v,176
s_waitcnt lgkmcnt(4)
CV 190,140,141
CV 191,142,143
MA 32,a,200,v,192
WR 238,188,4096
MA 32,a,208,v,196
s_waitcnt lgkmcnt(4)
s_add_u32 m0, s65, 0x18c00
MA 32,a,216,v,200
s_waitcnt vmcnt(7)
s_mov_b32 s53, 0x18c00
buffer_load_dwordx4 v249, s[44:47], s53 offen offset:0 nt lds
MC 132,v,112,a,64,-4.0
EX 128
MA 132,v,116,a,68
EX 129
MA 132,v,120,a,72
EX 130
MA 132,v,124,a,76
EX 131
MA 36,a,196,v,176
CV 176,128,129
CV 177,130,131
MA 36,a,204,v,192
RD a,64,236,40960
RD a,68,236,41472
MA 36,a,212,v,196
RD v,204,239,0
RD v,208,240,0
MA 36,a,220,v,200
RD v,212,241,0
RD a,72,236,43008
MC 136,v,96,a,80,-4.0
s_waitcnt lgkmcnt(9)
EX 132
MA 136,v,100,a,84
s_waitcnt lgkmcnt(8)
EX 133
MA 136,v,104,a,88
s_waitcnt lgkmcnt(7)
EX 134
MA 136,v,108,a,92
EX 135
MC 140,v,112,a,80,-4.0
CV 178,132,133
CV 179,134,135
MA 140,v,116,a,84
WR 238,176,8192
MA 140,v,120,a,88
s_add_u32 m0, s65, 0x19000
MA 140,v,124,a,92
s_waitcnt vmcnt(7)
)"
R"(s_mov_b32 s53, 0x19000
buffer_load_dwordx4 v249, s[44:47], s53 offen offset:0 nt lds
MC 216,v,220,v,180,0
EX 136
MA 216,v,224,v,188
s_waitcnt lgkmcnt(2)
s_barrier
MA 40,a,192,v,184
EX 137
MA 40,a,200,v,204
EX 138
MA 40,a,208,v,208
EX 139
MA 40,a,216,v,212
RD a,76,236,43520
RD a,80,236,45056
MA 44,a,196,v,184
CV 184,136,137
CV 185,138,139
MA 44,a,204,v,204
RD v,192,239,20480
RD v,196,240,20480
v_add_f32 v231, v231, v216
MA 44,a,212,v,208
RD v,200,241,20480
RD a,84,236,45568
MA 44,a,220,v,212
RD a,88,236,47104
RD a,92,236,47616
MC 128,v,96,a,64,-4.0
EX 140
MA 128,v,100,a,68
s_waitcnt lgkmcnt(9)
EX 141
MA 128,v,104,a,72
s_waitcnt lgkmcnt(7)
EX 142
MA 128,v,108,a,76
EX 143
MA 48,a,192,v,180
s_waitcnt lgkmcnt(4)
CV 186,140,141
CV 187,142,143
MA 48,a,200,v,192
WR 238,184,0
MA 48,a,208,v,196
s_waitcnt lgkmcnt(4)
s_add_u32 m0, s65, 0x19400
MA 48,a,216,v,200
s_waitcnt vmcnt(7)
s_mov_b32 s53, 0x19400
buffer_load_dwordx4 v249, s[44:47], s53 offen offset:0 nt lds
MC 132,v,112,a,64,-4.0
EX 128
MA 132,v,116,a,68
EX 129
MA 132,v,120,a,72
EX 130
MA 132,v,124,a,76
EX 131
MA 52,a,196,v,180
CV 180,128,129
CV 181,130,131
MA 52,a,204,v,192
RD a,64,236,0
RD a,68,236,512
MA 52,a,212,v,196
RD v,204,239,4096
RD v,208,240,4096
MA 52,a,220,v,200
RD v,212,241,4096
RD a,72,236,2048
MC 136,v,96,a,80,-4.0
s_waitcnt lgkmcnt(9)
EX 132
MA 136,v,100,a,84
s_waitcnt lgkmcnt(8)
EX 133
MA 136,v,104,a,88
s_waitcnt lgkmcnt(7)
EX 134
MA 136,v,108,a,92
EX 135
MC 140,v,112,a,80,-4.0
CV 182,132,133
CV 183,134,135
MA 140,v,116,a,84
WR 238,180,12288
MA 140,v,120,a,88
s_add_u32 m0, s65, 0x19800
MA 140,v,124,a,92
s_waitcnt vmcnt(7)
s_mov_b32 s53, 0x19800
buffer_load_dwordx4 v249, s[44:47], s53 offen offset:0 nt lds
MC 216,v,220,v,176,0
EX 136
MA 216,v,224,v,184
s_waitcnt lgkmcnt(2)
s_barrier
MA 56,a,192,v,188
EX 137
MA 56,a,200,v,204
EX 138
MA 56,a,208,v,208
EX 139
MA 56,a,216,v,212
RD a,76,236,2560
RD a,80,236,4096
MA 60,a,196,v,188
CV 188,136,137
CV 189,138,139
MA 60,a,204,v,204
RD v,192,239,8192
RD v,196,240,8192
v_add_f32 v232, v232, v216
MA 60,a,212,v,208
RD v,200,241,8192
RD a,84,236,4608
MA 60,a,220,v,212
RD a,88,236,6144
RD a,92,236,6656
v_lshlrev_b32 v252, 16, v144
v_lshlrev_b32 v254, 16, v145
v_and_b32 v253, 0xffff0000, v144
v_and_b32 v255, 0xffff0000, v145
v_pk_mul_f32 v[252:253], v[252:253], s[26:27]
v_pk_mul_f32 v[254:255], v[254:255], s[26:27]
CV 144,252,253
CV 145,254,255
v_lshlrev_b32 v252, 16, v146
v_lshlrev_b32 v254, 16, v147
v_and_b32 v253, 0xffff0000, v146
v_and_b32 v255, 0xffff0000, v147
v_pk_mul_f32 v[252:253], v[252:253], s[26:27]
v_pk_mul_f32 v[254:255], v[254:255], s[26:27]
CV 146,252,253
CV 147,254,255
v_lshlrev_b32 v252, 16, v148
v_lshlrev_b32 v254, 16, v149
v_and_b32 v253, 0xffff0000, v148
v_and_b32 v255, 0xffff0000, v149
v_pk_mul_f32 v[252:253], v[252:253], s[26:27]
v_pk_mul_f32 v[254:255], v[254:255], s[26:27]
CV 148,252,253
CV 149,254,255
v_lshlrev_b32 v252, 16, v150
v_lshlrev_b32 v254, 16, v151
v_and_b32 v253, 0xffff0000, v150
v_and_b32 v255, 0xffff0000, v151
v_pk_mul_f32 v[252:253], v[252:253], s[26:27]
v_pk_mul_f32 v[254:255], v[254:255], s[26:27]
CV 150,252,253
CV 151,254,255
v_lshlrev_b32 v252, 16, v152
v_lshlrev_b32 v254, 16, v153
v_and_b32 v253, 0xffff0000, v152
v_and_b32 v255, 0xffff0000, v153
v_pk_mul_f32 v[252:253], v[252:253], s[26:27]
v_pk_mul_f32 v[254:255], v[254:255], s[26:27]
CV 152,252,253
CV 153,254,255
v_lshlrev_b32 v252, 16, v154
v_lshlrev_b32 v254, 16, v155
v_and_b32 v253, 0xffff0000, v154
v_and_b32 v255, 0xffff0000, v155
v_pk_mul_f32 v[252:253], v[252:253], s[26:27]
v_pk_mul_f32 v[254:255], v[254:255], s[26:27]
CV 154,252,253
CV 155,254,255
v_lshlrev_b32 v252, 16, v156
v_lshlrev_b32 v254, 16, v157
v_and_b32 v253, 0xffff0000, v156
v_and_b32 v255, 0xffff0000, v157
v_pk_mul_f32 v[252:253], v[252:253], s[26:27]
v_pk_mul_f32 v[254:255], v[254:255], s[26:27]
CV 156,252,253
CV 157,254,255
v_lshlrev_b32 v252, 16, v158
v_lshlrev_b32 v254, 16, v159
v_and_b32 v253, 0xffff0000, v158
v_and_b32 v255, 0xffff0000, v159
v_pk_mul_f32 v[252:253], v[252:253], s[26:27]
v_pk_mul_f32 v[254:255], v[254:255], s[26:27]
CV 158,252,253
CV 159,254,255
v_lshlrev_b32 v252, 16, v160
v_lshlrev_b32 v254, 16, v161
v_and_b32 v253, 0xffff0000, v160
v_and_b32 v255, 0xffff0000, v161
v_pk_mul_f32 v[252:253], v[252:253], s[26:27]
v_pk_mul_f32 v[254:255], v[254:255], s[26:27]
CV 160,252,253
CV 161,254,255
v_lshlrev_b32 v252, 16, v162
v_lshlrev_b32 v254, 16, v163
v_and_b32 v253, 0xffff0000, v162
v_and_b32 v255, 0xffff0000, v163
v_pk_mul_f32 v[252:253], v[252:253], s[26:27]
v_pk_mul_f32 v[254:255], v[254:255], s[26:27]
CV 162,252,253
CV 163,254,255
v_lshlrev_b32 v252, 16, v164
v_lshlrev_b32 v254, 16, v165
v_and_b32 v253, 0xffff0000, v164
v_and_b32 v255, 0xffff0000, v165
v_pk_mul_f32 v[252:253], v[252:253], s[26:27]
v_pk_mul_f32 v[254:255], v[254:255], s[26:27]
CV 164,252,253
CV 165,254,255
v_lshlrev_b32 v252, 16, v166
v_lshlrev_b32 v254, 16, v167
v_and_b32 v253, 0xffff0000, v166
v_and_b32 v255, 0xffff0000, v167
v_pk_mul_f32 v[252:253], v[252:253], s[26:27]
v_pk_mul_f32 v[254:255], v[254:255], s[26:27]
CV 166,252,253
CV 167,254,255
v_lshlrev_b32 v252, 16, v168
v_lshlrev_b32 v254, 16, v169
v_and_b32 v253, 0xffff0000, v168
v_and_b32 v255, 0xffff0000, v169
v_pk_mul_f32 v[252:253], v[252:253], s[26:27]
v_pk_mul_f32 v[254:255], v[254:255], s[26:27]
CV 168,252,253
CV 169,254,255
v_lshlrev_b32 v252, 16, v170
v_lshlrev_b32 v254, 16, v171
v_and_b32 v253, 0xffff0000, v170
v_and_b32 v255, 0xffff0000, v171
v_pk_mul_f32 v[252:253], v[252:253], s[26:27]
v_pk_mul_f32 v[254:255], v[254:255], s[26:27]
CV 170,252,253
CV 171,254,255
s_waitcnt vmcnt(7)
v_lshlrev_b32 v252, 16, v172
v_lshlrev_b32 v254, 16, v173
v_and_b32 v253, 0xffff0000, v172
v_and_b32 v255, 0xffff0000, v173
v_pk_mul_f32 v[252:253], v[252:253], s[26:27]
v_pk_mul_f32 v[254:255], v[254:255], s[26:27]
CV 172,252,253
CV 173,254,255
v_lshlrev_b32 v252, 16, v174
v_lshlrev_b32 v254, 16, v175
v_and_b32 v253, 0xffff0000, v174
v_and_b32 v255, 0xffff0000, v175
v_pk_mul_f32 v[252:253], v[252:253], s[26:27]
v_pk_mul_f32 v[254:255], v[254:255], s[26:27]
CV 174,252,253
CV 175,254,255
MC 128,v,144,a,64,-4.0
EX 140
MA 128,v,148,a,68
s_waitcnt lgkmcnt(9)
EX 141
MA 128,v,152,a,72
s_waitcnt lgkmcnt(7)
EX 142
MA 128,v,156,a,76
EX 143
MA 64,a,192,v,176
s_waitcnt lgkmcnt(4)
CV 190,140,141
CV 191,142,143
MA 64,a,200,v,192
WR 238,188,4096
MA 64,a,208,v,196
s_waitcnt lgkmcnt(4)
s_add_u32 m0, s65, 0x19c00
MA 64,a,216,v,200
s_mov_b32 s53, 0x19c00
buffer_load_dwordx4 v249, s[44:47], s53 offen offset:0 nt lds
MC 132,v,160,a,64,-4.0
EX 128
MA 132,v,164,a,68
EX 129
MA 132,v,168,a,72
EX 130
MA 132,v,172,a,76
EX 131
MA 68,a,196,v,176
CV 176,128,129
CV 177,130,131
MA 68,a,204,v,192
RD a,64,236,8192
RD a,68,236,8704
MA 68,a,212,v,196
RD v,204,239,0
RD v,208,240,0
MA 68,a,220,v,200
RD v,212,241,0
RD a,72,236,10240
MC 136,v,144,a,80,-4.0
s_waitcnt lgkmcnt(9)
EX 132
MA 136,v,148,a,84
s_waitcnt lgkmcnt(8)
EX 133
MA 136,v,152,a,88
s_waitcnt lgkmcnt(7)
EX 134
MA 136,v,156,a,92
EX 135
MC 140,v,160,a,80,-4.0
CV 178,132,133
CV 179,134,135
MA 140,v,164,a,84
WR 238,176,16384
MA 140,v,168,a,88
s_add_u32 m0, s64, 0xc000
MA 140,v,172,a,92
s_waitcnt vmcnt(7)
s_mov_b32 s53, 0xc000
buffer_load_dwordx4 v237, s[36:39], s53 offen offset:0 lds
MC 216,v,220,v,180,0
EX 136
MA 216,v,224,v,188
s_waitcnt lgkmcnt(2)
s_barrier
MA 72,a,192,v,184
EX 137
MA 72,a,200,v,204
EX 138
MA 72,a,208,v,208
EX 139
MA 72,a,216,v,212
RD a,76,236,10752
RD a,80,236,12288
MA 76,a,196,v,184
CV 184,136,137
CV 185,138,139
MA 76,a,204,v,204
RD v,192,239,12288
RD v,196,240,12288
v_add_f32 v233, v233, v216
MA 76,a,212,v,208
RD v,200,241,12288
RD a,84,236,12800
MA 76,a,220,v,212
RD a,88,236,14336
RD a,92,236,14848
MC 128,v,144,a,64,-4.0
EX 140
MA 128,v,148,a,68
s_waitcnt lgkmcnt(9)
EX 141
MA 128,v,152,a,72
s_waitcnt lgkmcnt(7)
EX 142
MA 128,v,156,a,76
EX 143
MA 80,a,192,v,180
s_waitcnt lgkmcnt(4)
CV 186,140,141
CV 187,142,143
MA 80,a,200,v,192
WR 238,184,0
MA 80,a,208,v,196
s_waitcnt lgkmcnt(4)
s_add_u32 m0, s64, 0xc780
MA 80,a,216,v,200
s_waitcnt vmcnt(7)
s_mov_b32 s53, 0xc000
buffer_load_dwordx4 v237, s[36:39], s53 offen offset:128 lds
MC 132,v,160,a,64,-4.0
EX 128
MA 132,v,164,a,68
EX 129
MA 132,v,168,a,72
EX 130
MA 132,v,172,a,76
EX 131
MA 84,a,196,v,180
CV 180,128,129
CV 181,130,131
MA 84,a,204,v,192
RD a,64,236,16384
RD a,68,236,16896
MA 84,a,212,v,196
RD v,204,239,4096
RD v,208,240,4096
MA 84,a,220,v,200
RD v,212,241,4096
RD a,72,236,18432
MC 136,v,144,a,80,-4.0
s_waitcnt lgkmcnt(9)
EX 132
MA 136,v,148,a,84
s_waitcnt lgkmcnt(8)
EX 133
MA 136,v,152,a,88
s_waitcnt lgkmcnt(7)
EX 134
MA 136,v,156,a,92
EX 135
MC 140,v,160,a,80,-4.0
CV 182,132,133
CV 183,134,135
MA 140,v,164,a,84
WR 238,180,20480
)"
R"(MA 140,v,168,a,88
s_add_u32 m0, s64, 0xe000
MA 140,v,172,a,92
s_waitcnt vmcnt(7)
s_mov_b32 s53, 0xe000
buffer_load_dwordx4 v237, s[36:39], s53 offen offset:0 lds
MC 216,v,220,v,176,0
EX 136
MA 216,v,224,v,184
s_waitcnt lgkmcnt(2)
s_barrier
MA 88,a,192,v,188
EX 137
MA 88,a,200,v,204
EX 138
MA 88,a,208,v,208
EX 139
MA 88,a,216,v,212
RD a,76,236,18944
RD a,80,236,20480
MA 92,a,196,v,188
CV 188,136,137
CV 189,138,139
MA 92,a,204,v,204
RD v,192,239,16384
RD v,196,240,16384
v_add_f32 v228, v228, v216
MA 92,a,212,v,208
RD v,200,241,16384
RD a,84,236,20992
MA 92,a,220,v,212
RD a,88,236,22528
RD a,92,236,23040
s_add_u32 m0, s64, 0xe780
s_mov_b32 s53, 0xe000
s_waitcnt vmcnt(7)
buffer_load_dwordx4 v237, s[36:39], s53 offen offset:128 lds
s_add_u32 m0, s64, 0x10000
s_mov_b32 s53, 0x10000
s_waitcnt vmcnt(7)
buffer_load_dwordx4 v237, s[36:39], s53 offen offset:0 lds
s_add_u32 m0, s64, 0x10780
s_mov_b32 s53, 0x10000
s_waitcnt vmcnt(7)
buffer_load_dwordx4 v237, s[36:39], s53 offen offset:128 lds
s_add_u32 m0, s64, 0x12000
s_mov_b32 s53, 0x12000
s_waitcnt vmcnt(7)
buffer_load_dwordx4 v237, s[36:39], s53 offen offset:0 lds
s_waitcnt vmcnt(7)
s_barrier
v_add_u32 v252, s56, v250
v_add_u32 v253, s57, v250
v_add_u32 v254, s58, v250
v_add_u32 v255, s59, v250
ds_read_b64_tr_b16 a[224:225], v252 offset:32768
ds_read_b64_tr_b16 a[226:227], v252 offset:36864
ds_read_b64_tr_b16 a[232:233], v253 offset:32768
ds_read_b64_tr_b16 a[234:235], v253 offset:36864
ds_read_b64_tr_b16 a[240:241], v254 offset:32768
s_waitcnt lgkmcnt(14)
ds_read_b64_tr_b16 a[242:243], v254 offset:36864
s_waitcnt lgkmcnt(14)
ds_read_b64_tr_b16 a[248:249], v255 offset:32768
s_waitcnt lgkmcnt(14)
ds_read_b64_tr_b16 a[250:251], v255 offset:36864
s_waitcnt lgkmcnt(14)
ds_read_b64_tr_b16 a[228:229], v252 offset:32776
s_waitcnt lgkmcnt(14)
ds_read_b64_tr_b16 a[230:231], v252 offset:36872
s_waitcnt lgkmcnt(14)
ds_read_b64_tr_b16 a[236:237], v253 offset:32776
s_waitcnt lgkmcnt(14)
ds_read_b64_tr_b16 a[238:239], v253 offset:36872
s_waitcnt lgkmcnt(14)
ds_read_b64_tr_b16 a[244:245], v254 offset:32776
s_waitcnt lgkmcnt(14)
ds_read_b64_tr_b16 a[246:247], v254 offset:36872
s_waitcnt lgkmcnt(14)
ds_read_b64_tr_b16 a[252:253], v255 offset:32776
s_waitcnt lgkmcnt(14)
ds_read_b64_tr_b16 a[254:255], v255 offset:36872
MC 128,v,144,a,64,-4.0
EX 140
MA 128,v,148,a,68
EX 141
MA 128,v,152,a,72
EX 142
MA 128,v,156,a,76
s_waitcnt lgkmcnt(14)
EX 143
MA 0,a,224,v,176
s_waitcnt lgkmcnt(12)
CV 190,140,141
CV 191,142,143
MA 0,a,232,v,192
s_waitcnt lgkmcnt(10)
WR 238,188,4096
MA 0,a,240,v,196
s_waitcnt lgkmcnt(9)
s_add_u32 m0, s64, 0x12780
MA 0,a,248,v,200
s_mov_b32 s53, 0x12000
buffer_load_dwordx4 v237, s[36:39], s53 offen offset:128 lds
MC 132,v,160,a,64,-4.0
EX 128
MA 132,v,164,a,68
EX 129
MA 132,v,168,a,72
EX 130
MA 132,v,172,a,76
s_waitcnt lgkmcnt(7)
EX 131
MA 4,a,228,v,176
s_waitcnt lgkmcnt(5)
CV 176,128,129
CV 177,130,131
MA 4,a,236,v,192
s_waitcnt lgkmcnt(3)
RD a,64,236,24576
RD a,68,236,25088
MA 4,a,244,v,196
s_waitcnt lgkmcnt(3)
RD v,204,239,0
RD v,208,240,0
ds_write_b32 v242, v228 offset:0
MA 4,a,252,v,200
RD v,212,241,0
RD a,72,236,26624
MC 136,v,144,a,80,-4.0
EX 132
MA 136,v,148,a,84
EX 133
MA 136,v,152,a,88
EX 134
MA 136,v,156,a,92
EX 135
MC 140,v,160,a,80,-4.0
CV 178,132,133
CV 179,134,135
MA 140,v,164,a,84
WR 238,176,8192
MA 140,v,168,a,88
s_add_u32 m0, s64, 0x14000
MA 140,v,172,a,92
s_waitcnt vmcnt(7)
s_mov_b32 s53, 0x14000
buffer_load_dwordx4 v237, s[36:39], s53 offen offset:0 lds
MC 216,v,220,v,180,0
EX 136
MA 216,v,224,v,188
s_waitcnt lgkmcnt(2)
s_barrier
MA 8,a,224,v,184
EX 137
MA 8,a,232,v,204
EX 138
MA 8,a,240,v,208
EX 139
MA 8,a,248,v,212
RD a,76,236,27136
RD a,80,236,28672
MA 12,a,228,v,184
CV 184,136,137
CV 185,138,139
MA 12,a,236,v,204
RD v,192,239,20480
RD v,196,240,20480
RD v,252,243,0
v_add_f32 v229, v229, v216
MA 12,a,244,v,208
RD v,200,241,20480
RD a,84,236,29184
MA 12,a,252,v,212
RD a,88,236,30720
RD a,92,236,31232
MC 128,v,144,a,64,-4.0
EX 140
MA 128,v,148,a,68
s_waitcnt lgkmcnt(10)
EX 141
MA 128,v,152,a,72
s_waitcnt lgkmcnt(8)
EX 142
MA 128,v,156,a,76
s_waitcnt lgkmcnt(4)
EX 143
v_add_f32 v252, v252, v253
v_add_f32 v254, v254, v255
MA 16,a,224,v,180
CV 186,140,141
CV 187,142,143
v_add_f32 v252, v252, v254
MA 16,a,232,v,192
WR 238,184,0
MA 16,a,240,v,196
s_waitcnt lgkmcnt(4)
s_add_u32 m0, s64, 0x14780
v_rcp_f32 v253, v252
s_nop 0
v_maximum3_f32 v246, v246, v252, v253
v_mov_b32 v254, v253
MA 16,a,248,v,200
s_waitcnt vmcnt(7)
s_mov_b32 s53, 0x14000
buffer_load_dwordx4 v237, s[36:39], s53 offen offset:128 lds
MC 132,v,160,a,64,-4.0
EX 128
v_permlane32_swap_b32 v253, v254
MA 132,v,164,a,68
EX 129
MU 0,253
MU 1,253
MA 132,v,168,a,72
EX 130
MU 2,253
MU 3,253
MA 132,v,172,a,76
EX 131
MU 4,253
MU 5,253
MA 20,a,228,v,180
CV 180,128,129
CV 181,130,131
MU 6,253
MU 7,253
MA 20,a,236,v,192
RD a,64,236,32768
RD a,68,236,33280
CV 0,0,1
CV 1,2,3
MA 20,a,244,v,196
RD v,204,239,4096
RD v,208,240,4096
ds_write_b32 v242, v229 offset:512
CV 2,4,5
CV 3,6,7
MA 20,a,252,v,200
RD v,212,241,4096
RD a,72,236,34816
MU 8,254
MC 136,v,144,a,80,-4.0
s_waitcnt lgkmcnt(10)
EX 132
MU 9,254
MA 136,v,148,a,84
s_waitcnt lgkmcnt(9)
EX 133
MU 10,254
MA 136,v,152,a,88
s_waitcnt lgkmcnt(8)
EX 134
MU 11,254
MA 136,v,156,a,92
EX 135
MU 12,254
MC 140,v,160,a,80,-4.0
CV 182,132,133
CV 183,134,135
MU 13,254
MA 140,v,164,a,84
WR 238,180,12288
MA 140,v,168,a,88
s_add_u32 m0, s64, 0x16000
MU 14,254
MU 15,254
CV 8,8,9
MA 140,v,172,a,92
s_waitcnt vmcnt(7)
s_mov_b32 s53, 0x16000
buffer_load_dwordx4 v237, s[36:39], s53 offen offset:0 lds
MC 216,v,220,v,176,0
EX 136
CV 9,10,11
MA 216,v,224,v,184
s_waitcnt lgkmcnt(2)
s_barrier
MA 24,a,224,v,188
s_mov_b32 s67, 0
EX 137
CV 10,12,13
MA 24,a,232,v,204
EX 138
CV 11,14,15
MA 24,a,240,v,208
EX 139
MA 24,a,248,v,212
RD a,76,236,35328
RD a,80,236,36864
buffer_store_dwordx4 v[0:3], v244, s[48:51], s67 offen offset:0
MA 28,a,228,v,188
CV 188,136,137
CV 189,138,139
MA 28,a,236,v,204
RD v,192,239,8192
RD v,196,240,8192
RD v,252,243,512
v_add_f32 v230, v230, v216
MA 28,a,244,v,208
RD v,200,241,8192
RD a,84,236,37376
MA 28,a,252,v,212
RD a,88,236,38912
RD a,92,236,39424
buffer_store_dwordx4 v[8:11], v245, s[48:51], s67 offen offset:0
MC 128,v,144,a,64,-4.0
EX 140
MA 128,v,148,a,68
s_waitcnt lgkmcnt(10)
EX 141
MA 128,v,152,a,72
s_waitcnt lgkmcnt(8)
EX 142
MA 128,v,156,a,76
s_waitcnt lgkmcnt(4)
EX 143
v_add_f32 v252, v252, v253
v_add_f32 v254, v254, v255
MA 32,a,224,v,176
CV 190,140,141
CV 191,142,143
v_add_f32 v252, v252, v254
MA 32,a,232,v,192
WR 238,188,4096
MA 32,a,240,v,196
s_waitcnt lgkmcnt(4)
s_add_u32 m0, s64, 0x16780
v_rcp_f32 v253, v252
s_nop 0
v_maximum3_f32 v246, v246, v252, v253
v_mov_b32 v254, v253
MA 32,a,248,v,200
s_waitcnt vmcnt(9)
s_mov_b32 s53, 0x16000
buffer_load_dwordx4 v237, s[36:39], s53 offen offset:128 lds
MC 132,v,160,a,64,-4.0
EX 128
v_permlane32_swap_b32 v253, v254
MA 132,v,164,a,68
EX 129
MU 16,253
MU 17,253
MA 132,v,168,a,72
EX 130
MU 18,253
MU 19,253
MA 132,v,172,a,76
EX 131
MU 20,253
MU 21,253
MA 36,a,228,v,176
CV 176,128,129
CV 177,130,131
MU 22,253
MU 23,253
MA 36,a,236,v,192
RD a,64,236,40960
RD a,68,236,41472
CV 16,16,17
CV 17,18,19
MA 36,a,244,v,196
RD v,204,239,0
RD v,208,240,0
ds_write_b32 v242, v230 offset:1024
CV 18,20,21
CV 19,22,23
MA 36,a,252,v,200
RD v,212,241,0
RD a,72,236,43008
MU 24,254
MC 136,v,144,a,80,-4.0
s_waitcnt lgkmcnt(10)
EX 132
MU 25,254
MA 136,v,148,a,84
s_waitcnt lgkmcnt(9)
EX 133
MU 26,254
MA 136,v,152,a,88
s_waitcnt lgkmcnt(8)
EX 134
MU 27,254
MA 136,v,156,a,92
EX 135
MU 28,254
MC 140,v,160,a,80,-4.0
CV 178,132,133
CV 179,134,135
MU 29,254
MA 140,v,164,a,84
WR 238,176,16384
MA 140,v,168,a,88
MU 30,254
MU 31,254
CV 24,24,25
MA 140,v,172,a,92
CV 25,26,27
CV 26,28,29
CV 27,30,31
MC 216,v,220,v,180,0
)"
R"(EX 136
MA 216,v,224,v,188
s_waitcnt lgkmcnt(2)
s_barrier
MA 40,a,224,v,184
s_mov_b32 s67, 0x2000
EX 137
MA 40,a,232,v,204
EX 138
MA 40,a,240,v,208
EX 139
MA 40,a,248,v,212
RD a,76,236,43520
RD a,80,236,45056
buffer_store_dwordx4 v[16:19], v244, s[48:51], s67 offen offset:0
MA 44,a,228,v,184
CV 184,136,137
CV 185,138,139
MA 44,a,236,v,204
RD v,192,239,12288
RD v,196,240,12288
RD v,252,243,1024
v_add_f32 v231, v231, v216
MA 44,a,244,v,208
RD v,200,241,12288
RD a,84,236,45568
MA 44,a,252,v,212
RD a,88,236,47104
RD a,92,236,47616
buffer_store_dwordx4 v[24:27], v245, s[48:51], s67 offen offset:0
CP23_%=:
s_add_u32 s53, s24, CP23_%=-PCREF%=
s_lshr_b32 s55, s65, 3
s_add_u32 m0, s55, 0x27000
s_nop 0
buffer_load_dwordx4 v238, s[20:23], s53 offen lds
MC 128,v,144,a,64,-4.0
EX 140
MA 128,v,148,a,68
s_waitcnt lgkmcnt(10)
EX 141
MA 128,v,152,a,72
s_waitcnt lgkmcnt(8)
EX 142
MA 128,v,156,a,76
s_waitcnt lgkmcnt(4)
EX 143
v_add_f32 v252, v252, v253
MA 48,a,224,v,180
CV 186,140,141
CV 187,142,143
v_add_f32 v254, v254, v255
MA 48,a,232,v,192
WR 238,184,0
MA 48,a,240,v,196
s_waitcnt lgkmcnt(4)
v_add_f32 v252, v252, v254
v_rcp_f32 v253, v252
MA 48,a,248,v,200
v_maximum3_f32 v246, v246, v252, v253
v_mov_b32 v254, v253
s_nop 1
v_permlane32_swap_b32 v253, v254
MC 132,v,160,a,64,-4.0
EX 128
MU 32,253
MA 132,v,164,a,68
EX 129
MU 33,253
MA 132,v,168,a,72
EX 130
MU 34,253
MA 132,v,172,a,76
EX 131
MU 35,253
MA 52,a,228,v,180
CV 180,128,129
CV 181,130,131
MU 36,253
MA 52,a,236,v,192
RD a,64,236,49152
RD a,68,236,49664
MU 37,253
MA 52,a,244,v,196
RD v,204,239,4096
RD v,208,240,4096
ds_write_b32 v242, v231 offset:1536
MU 38,253
MA 52,a,252,v,200
RD v,212,241,4096
RD a,72,236,51200
MU 39,253
MC 136,v,144,a,80,-4.0
s_waitcnt lgkmcnt(10)
EX 132
CV 32,32,33
MA 136,v,148,a,84
s_waitcnt lgkmcnt(9)
EX 133
CV 33,34,35
MA 136,v,152,a,88
s_waitcnt lgkmcnt(8)
EX 134
CV 34,36,37
MA 136,v,156,a,92
EX 135
CV 35,38,39
MC 140,v,160,a,80,-4.0
CV 182,132,133
CV 183,134,135
MU 40,254
MU 41,254
MA 140,v,164,a,84
WR 238,180,20480
MA 140,v,168,a,88
MU 42,254
MU 43,254
MU 44,254
MU 45,254
MA 140,v,172,a,92
MU 46,254
MU 47,254
CV 40,40,41
CV 41,42,43
MC 216,v,220,v,176,0
EX 136
CV 42,44,45
CV 43,46,47
MA 216,v,224,v,184
s_waitcnt lgkmcnt(2)
s_barrier
MA 56,a,224,v,188
s_mov_b32 s67, 0x4000
EX 137
MA 56,a,232,v,204
EX 138
MA 56,a,240,v,208
EX 139
MA 56,a,248,v,212
RD a,76,236,51712
RD a,80,236,53248
buffer_store_dwordx4 v[32:35], v244, s[48:51], s67 offen offset:0
MA 60,a,228,v,188
CV 188,136,137
CV 189,138,139
MA 60,a,236,v,204
RD v,192,239,16384
RD v,196,240,16384
RD v,252,243,1536
v_add_f32 v232, v232, v216
MA 60,a,244,v,208
RD v,200,241,16384
RD a,84,236,53760
MA 60,a,252,v,212
RD a,88,236,55296
RD a,92,236,55808
buffer_store_dwordx4 v[40:43], v245, s[48:51], s67 offen offset:0
s_mov_b32 s60, 0x18000
s_add_u32 s61, s64, 0
s_add_u32 s66, s64, 0x18000
s_mov_b32 s63, 0xc000
s_mov_b32 s62, 0xa000
v_mov_b32 v40, v235
v_mov_b32 v41, v235
v_mov_b32 v42, v235
v_mov_b32 v43, v235
v_mov_b32 v44, 1.0
v_mov_b32 v45, 1.0
v_mov_b32 v46, 1.0
MC 128,a,0,a,64,-4.0
EX 140
MA 128,a,4,a,68
s_waitcnt lgkmcnt(10)
EX 141
MA 128,a,8,a,72
s_waitcnt lgkmcnt(8)
EX 142
MA 128,a,12,a,76
s_waitcnt lgkmcnt(4)
EX 143
v_add_f32 v252, v252, v253
MA 64,a,224,v,176
CV 190,140,141
CV 191,142,143
v_add_f32 v254, v254, v255
MA 64,a,232,v,192
WR 238,188,4096
MA 64,a,240,v,196
s_waitcnt lgkmcnt(4)
v_add_u32 v247, s63, v236
v_add_f32 v252, v252, v254
MA 64,a,248,v,200
v_rcp_f32 v253, v252
s_nop 0
v_maximum3_f32 v246, v246, v252, v253
MC 132,a,16,a,64,-4.0
s_add_u32 s55, s63, 0x4000
EX 128
v_mov_b32 v254, v253
MA 132,a,20,a,68
s_cmp_lt_u32 s55, 0x18000
EX 129
v_permlane32_swap_b32 v253, v254
MA 132,a,24,a,72
s_cselect_b32 s63, s55, 0
EX 130
MU 48,253
MA 132,a,28,a,76
EX 131
MU 49,253
MA 68,a,228,v,176
CV 176,128,129
CV 177,130,131
MU 50,253
MA 68,a,236,v,192
MU 51,253
MU 52,253
MU 53,253
MA 68,a,244,v,196
RD v,204,239,0
RD v,208,240,0
ds_write_b32 v242, v232 offset:0
MU 54,253
MA 68,a,252,v,200
RD v,212,241,0
RD a,96,247,8192
MU 55,253
MC 136,a,0,a,80,-4.0
s_waitcnt lgkmcnt(8)
EX 132
CV 48,48,49
MA 136,a,4,a,84
s_waitcnt lgkmcnt(7)
EX 133
CV 49,50,51
MA 136,a,8,a,88
s_waitcnt lgkmcnt(6)
EX 134
CV 50,52,53
MA 136,a,12,a,92
EX 135
CV 51,54,55
MC 140,a,16,a,80,-4.0
CV 178,132,133
CV 179,134,135
MU 56,254
MU 57,254
MA 140,a,20,a,84
WR 238,176,8192
MA 140,a,24,a,88
MU 58,254
MU 59,254
MU 60,254
MU 61,254
MA 140,a,28,a,92
MU 62,254
MU 63,254
CV 56,56,57
CV 57,58,59
MC 216,v,220,v,180,0
EX 136
CV 58,60,61
CV 59,62,63
MA 216,v,224,v,188
s_waitcnt lgkmcnt(2)
s_barrier
MA 72,a,224,v,184
s_mov_b32 s67, 0x6000
EX 137
MA 72,a,232,v,204
EX 138
MA 72,a,240,v,208
EX 139
MA 72,a,248,v,212
buffer_store_dwordx4 v[48:51], v244, s[48:51], s67 offen offset:0
MA 76,a,228,v,184
CV 184,136,137
CV 185,138,139
MA 76,a,236,v,204
RD v,192,239,20480
RD v,196,240,20480
RD v,252,243,0
v_add_f32 v233, v233, v216
MA 76,a,244,v,208
RD v,200,241,20480
RD a,100,247,8704
MA 76,a,252,v,212
buffer_store_dwordx4 v[56:59], v245, s[48:51], s67 offen offset:0
MC 128,a,32,a,64,-4.0
EX 140
MA 128,a,36,a,68
EX 141
MA 128,a,40,a,72
EX 142
MA 128,a,44,a,76
s_waitcnt lgkmcnt(2)
EX 143
v_add_f32 v252, v252, v253
MA 80,a,224,v,180
CV 186,140,141
CV 187,142,143
v_add_f32 v254, v254, v255
MA 80,a,232,v,192
WR 238,184,0
MA 80,a,240,v,196
s_waitcnt lgkmcnt(2)
v_mov_b32 v216, 0
v_mov_b32 v217, 0
v_add_f32 v252, v252, v254
MA 80,a,248,v,200
v_rcp_f32 v253, v252
s_nop 0
v_maximum3_f32 v246, v246, v252, v253
MC 132,a,48,a,64,-4.0
EX 128
v_mov_b32 v254, v253
MA 132,a,52,a,68
EX 129
v_permlane32_swap_b32 v253, v254
MA 132,a,56,a,72
EX 130
MU 64,253
MA 132,a,60,a,76
EX 131
MU 65,253
MA 84,a,228,v,180
CV 180,128,129
CV 181,130,131
MU 66,253
MA 84,a,236,v,192
MU 67,253
MU 68,253
MU 69,253
MA 84,a,244,v,196
RD v,204,239,4096
RD v,208,240,4096
ds_write_b32 v242, v233 offset:512
MU 70,253
MA 84,a,252,v,200
RD v,212,241,4096
RD a,104,247,10240
MU 71,253
MC 136,a,32,a,80,-4.0
EX 132
CV 64,64,65
MA 136,a,36,a,84
EX 133
CV 65,66,67
MA 136,a,40,a,88
EX 134
CV 66,68,69
MA 136,a,44,a,92
EX 135
CV 67,70,71
MC 140,a,48,a,80,-4.0
CV 182,132,133
CV 183,134,135
MU 72,254
MU 73,254
MA 140,a,52,a,84
WR 238,180,12288
MA 140,a,56,a,88
MU 74,254
MU 75,254
MU 76,254
MU 77,254
MA 140,a,60,a,92
MU 78,254
MU 79,254
CV 72,72,73
CV 73,74,75
MA 88,a,224,v,188
EX 136
CV 74,76,77
CV 75,78,79
v_smfmac_f32_16x16x64_bf16 v[216:219], v[220:223], v[176:183], v234
s_waitcnt lgkmcnt(2)
s_barrier
MA 88,a,232,v,204
s_mov_b32 s67, 0x8000
EX 137
MA 88,a,240,v,208
EX 138
MA 88,a,248,v,212
EX 139
MA 92,a,228,v,188
buffer_store_dwordx4 v[64:67], v244, s[48:51], s67 offen offset:0
CV 188,136,137
CV 189,138,139
MA 92,a,236,v,204
RD v,192,239,8192
RD v,196,240,8192
RD v,252,243,512
MA 92,a,244,v,208
RD v,200,241,8192
RD a,108,247,10752
MA 92,a,252,v,212
buffer_store_dwordx4 v[72:75], v245, s[48:51], s67 offen offset:0
MC 128,v,96,a,64,-4.0
EX 140
)"
R"(MA 128,v,100,a,68
EX 141
MA 128,v,104,a,72
EX 142
MA 128,v,108,a,76
s_waitcnt lgkmcnt(2)
EX 143
v_add_f32 v252, v252, v253
MR 0,a,128,v,176,40
CV 190,140,141
CV 191,142,143
v_add_f32 v254, v254, v255
MA 0,a,136,v,192
WR 238,188,4096
MA 0,a,144,v,196
s_waitcnt lgkmcnt(2)
v_add_f32 v252, v252, v254
v_rcp_f32 v253, v252
MA 0,a,152,v,200
v_maximum3_f32 v246, v246, v252, v253
v_mov_b32 v254, v253
s_nop 1
v_permlane32_swap_b32 v253, v254
MC 132,v,112,a,64,-4.0
EX 128
MU 80,253
MA 132,v,116,a,68
EX 129
MU 81,253
MA 132,v,120,a,72
EX 130
MU 82,253
MA 132,v,124,a,76
EX 131
MU 83,253
MR 4,a,132,v,176,40
CV 176,128,129
CV 177,130,131
MU 84,253
MA 4,a,140,v,192
MU 85,253
MU 86,253
MU 87,253
MA 4,a,148,v,196
RD v,204,239,0
RD v,208,240,0
CV 80,80,81
MA 4,a,156,v,200
RD v,212,241,0
RD a,112,247,12288
CV 81,82,83
MC 136,v,96,a,80,-4.0
EX 132
CV 82,84,85
MA 136,v,100,a,84
EX 133
CV 83,86,87
MA 136,v,104,a,88
EX 134
MU 88,254
MA 136,v,108,a,92
EX 135
MU 89,254
MC 140,v,112,a,80,-4.0
CV 178,132,133
CV 179,134,135
MU 90,254
MA 140,v,116,a,84
WR 238,176,16384
MA 140,v,120,a,88
MU 91,254
MU 92,254
MU 93,254
MA 140,v,124,a,92
MU 94,254
MU 95,254
CV 88,88,89
MR 8,a,128,v,184,40
EX 136
CV 89,90,91
v_smfmac_f32_16x16x64_bf16 v[216:219], v[224:227], v[184:191], v234
s_waitcnt lgkmcnt(2)
s_barrier
MA 8,a,136,v,204
s_mov_b32 s67, 0xa000
EX 137
CV 90,92,93
MA 8,a,144,v,208
EX 138
CV 91,94,95
MA 8,a,152,v,212
EX 139
MR 12,a,132,v,184,40
buffer_store_dwordx4 v[80:83], v244, s[48:51], s67 offen offset:0
CV 184,136,137
CV 185,138,139
MA 12,a,140,v,204
RD v,192,239,12288
RD v,196,240,12288
MA 12,a,148,v,208
RD v,200,241,12288
RD a,116,247,12800
MA 12,a,156,v,212
buffer_store_dwordx4 v[88:91], v245, s[48:51], s67 offen offset:0
MC 128,v,144,a,64,-4.0
EX 140
MA 128,v,148,a,68
EX 141
MA 128,v,152,a,72
EX 142
MA 128,v,156,a,76
EX 143
MA 0,a,160,v,180
s_waitcnt lgkmcnt(2)
CV 186,140,141
CV 187,142,143
MA 0,a,168,v,192
WR 238,184,0
MA 0,a,176,v,196
s_waitcnt lgkmcnt(2)
MA 0,a,184,v,200
MC 132,v,160,a,64,-4.0
EX 128
MA 132,v,164,a,68
EX 129
MA 132,v,168,a,72
EX 130
MA 132,v,172,a,76
EX 131
MA 4,a,164,v,180
CV 180,128,129
CV 181,130,131
MA 4,a,172,v,192
MA 4,a,180,v,196
RD v,204,239,4096
RD v,208,240,4096
MA 4,a,188,v,200
RD v,212,241,4096
RD a,120,247,14336
MC 136,v,144,a,80,-4.0
EX 132
MA 136,v,148,a,84
EX 133
MA 136,v,152,a,88
EX 134
MA 136,v,156,a,92
EX 135
MC 140,v,160,a,80,-4.0
CV 182,132,133
CV 183,134,135
MA 140,v,164,a,84
WR 238,180,20480
MA 140,v,168,a,88
MA 140,v,172,a,92
MA 8,a,160,v,188
EX 136
v_smfmac_f32_16x16x64_bf16 v[216:219], v[220:223], v[176:183], v234
s_waitcnt vmcnt(19) lgkmcnt(2)
s_barrier
MA 8,a,168,v,204
EX 137
MA 8,a,176,v,208
EX 138
MA 8,a,184,v,212
EX 139
MA 12,a,164,v,188
CV 188,136,137
CV 189,138,139
MA 12,a,172,v,204
RD v,192,239,16384
RD v,196,240,16384
MA 12,a,180,v,208
s_add_u32 m0, s61, 0
RD v,200,241,16384
RD a,124,247,14848
MA 12,a,188,v,212
buffer_load_dwordx4 v237, s[36:39], s60 offen offset:0 lds
MC 128,a,0,a,96,-4.0
EX 140
MA 128,a,4,a,100
EX 141
MA 128,a,8,a,104
EX 142
MA 128,a,12,a,108
EX 143
MA 0,a,192,v,176
s_waitcnt lgkmcnt(2)
CV 190,140,141
CV 191,142,143
MA 0,a,200,v,192
WR 238,188,4096
MA 0,a,208,v,196
s_waitcnt lgkmcnt(1)
v_add_u32 v247, s63, v236
MA 0,a,216,v,200
MC 132,a,16,a,96,-4.0
s_add_u32 s55, s63, 0x4000
EX 128
MA 132,a,20,a,100
s_cmp_lt_u32 s55, 0x18000
EX 129
MA 132,a,24,a,104
s_cselect_b32 s63, s55, 0
EX 130
MA 132,a,28,a,108
EX 131
MA 4,a,196,v,176
s_add_u32 m0, s61, 0x780
CV 176,128,129
CV 177,130,131
MA 4,a,204,v,192
buffer_load_dwordx4 v237, s[36:39], s60 offen offset:128 lds
MA 4,a,212,v,196
RD v,204,239,0
RD v,208,240,0
MA 4,a,220,v,200
RD v,212,241,0
RD a,64,247,0
MC 136,a,0,a,112,-4.0
s_add_u32 s55, s61, 0x2000
EX 132
MA 136,a,4,a,116
s_cmp_lt_u32 s55, s66
EX 133
MA 136,a,8,a,120
s_cselect_b32 s61, s55, s64
EX 134
MA 136,a,12,a,124
s_add_u32 s60, s60, 0x2000
EX 135
MC 140,a,16,a,112,-4.0
CV 178,132,133
CV 179,134,135
MA 140,a,20,a,116
WR 238,176,8192
MA 140,a,24,a,120
MA 140,a,28,a,124
MA 8,a,192,v,184
EX 136
v_smfmac_f32_16x16x64_bf16 v[216:219], v[224:227], v[184:191], v234
s_waitcnt lgkmcnt(2)
s_barrier
MA 8,a,200,v,204
EX 137
MA 8,a,208,v,208
EX 138
MA 8,a,216,v,212
EX 139
MA 12,a,196,v,184
CV 184,136,137
CV 185,138,139
MA 12,a,204,v,204
RD v,192,239,20480
RD v,196,240,20480
MA 12,a,212,v,208
RD v,200,241,20480
RD a,68,247,512
MA 12,a,220,v,212
v_add_f32 v235, v216, v217
v_mov_b64 v[216:217], 0
MC 128,a,32,a,96,-4.0
EX 140
MA 128,a,36,a,100
EX 141
MA 128,a,40,a,104
EX 142
MA 128,a,44,a,108
EX 143
MA 0,a,224,v,180
s_waitcnt lgkmcnt(2)
CV 186,140,141
CV 187,142,143
MA 0,a,232,v,192
WR 238,184,0
MA 0,a,240,v,196
s_waitcnt lgkmcnt(2)
MA 0,a,248,v,200
MC 132,a,48,a,96,-4.0
EX 128
MA 132,a,52,a,100
EX 129
MA 132,a,56,a,104
EX 130
MA 132,a,60,a,108
EX 131
MA 4,a,228,v,180
CV 180,128,129
CV 181,130,131
MA 4,a,236,v,192
ds_write_b32 v242, v235 offset:0
MA 4,a,244,v,196
RD v,204,239,4096
RD v,208,240,4096
MA 4,a,252,v,200
RD v,212,241,4096
RD a,72,247,2048
MC 136,a,32,a,112,-4.0
EX 132
MA 136,a,36,a,116
EX 133
MA 136,a,40,a,120
EX 134
MA 136,a,44,a,124
EX 135
MC 140,a,48,a,112,-4.0
CV 182,132,133
CV 183,134,135
MA 140,a,52,a,116
WR 238,180,12288
MA 140,a,56,a,120
MA 140,a,60,a,124
MA 8,a,224,v,188
EX 136
v_smfmac_f32_16x16x64_bf16 v[216:219], v[220:223], v[176:183], v234
s_waitcnt lgkmcnt(2)
s_barrier
MA 8,a,232,v,204
EX 137
MA 8,a,240,v,208
EX 138
MA 8,a,248,v,212
EX 139
MA 12,a,228,v,188
CV 188,136,137
CV 189,138,139
MA 12,a,236,v,204
RD v,192,239,8192
RD v,196,240,8192
MA 12,a,244,v,208
RD v,200,241,8192
RD a,76,247,2560
MA 12,a,252,v,212
RD v,252,243,0
MC 128,v,96,a,96,-4.0
EX 140
MA 128,v,100,a,100
EX 141
MA 128,v,104,a,104
EX 142
MA 128,v,108,a,108
EX 143
MR 16,a,128,v,176,40
s_waitcnt lgkmcnt(3)
CV 190,140,141
CV 191,142,143
MA 16,a,136,v,192
WR 238,188,4096
MA 16,a,144,v,196
s_waitcnt lgkmcnt(1)
v_add_f32 v252, v252, v253
v_add_f32 v254, v254, v255
MA 16,a,152,v,200
v_add_f32 v44, v252, v254
MC 132,v,112,a,96,-4.0
EX 128
MA 132,v,116,a,100
EX 129
MA 132,v,120,a,104
EX 130
MA 132,v,124,a,108
EX 131
MR 20,a,132,v,176,40
CV 176,128,129
CV 177,130,131
MA 20,a,140,v,192
MA 20,a,148,v,196
RD v,204,239,0
RD v,208,240,0
MA 20,a,156,v,200
RD v,212,241,0
RD a,80,247,4096
MC 136,v,96,a,112,-4.0
EX 132
MA 136,v,100,a,116
EX 133
MA 136,v,104,a,120
EX 134
MA 136,v,108,a,124
EX 135
MC 140,v,112,a,112,-4.0
CV 178,132,133
CV 179,134,135
MA 140,v,116,a,116
WR 238,176,16384
MA 140,v,120,a,120
v_rcp_f32 v253, v44
MA 140,v,124,a,124
v_maximum3_f32 v246, v246, v44, v45
v_mov_b32 v254, v253
MR 24,a,128,v,184,40
EX 136
v_smfmac_f32_16x16x64_bf16 v[216:219], v[224:227], v[184:191], v234
s_waitcnt lgkmcnt(2)
s_barrier
MA 24,a,136,v,204
EX 137
MA 24,a,144,v,208
EX 138
MA 24,a,152,v,212
EX 139
)"
R"(MR 28,a,132,v,184,40
CV 184,136,137
CV 185,138,139
MA 28,a,140,v,204
RD v,192,239,12288
RD v,196,240,12288
MA 28,a,148,v,208
RD v,200,241,12288
RD a,84,247,4608
MA 28,a,156,v,212
v_permlane32_swap_b32 v253, v254
MC 128,v,144,a,96,-4.0
EX 140
MA 128,v,148,a,100
EX 141
MA 128,v,152,a,104
EX 142
MA 128,v,156,a,108
EX 143
MA 16,a,160,v,180
s_waitcnt lgkmcnt(2)
CV 186,140,141
CV 187,142,143
MA 16,a,168,v,192
WR 238,184,0
MA 16,a,176,v,196
s_waitcnt lgkmcnt(2)
v_mul_f32 v252, -v40, v253
FM 0,253,252
MA 16,a,184,v,200
FM 1,253,252
FM 2,253,252
MC 132,v,160,a,96,-4.0
EX 128
MA 132,v,164,a,100
EX 129
MA 132,v,168,a,104
EX 130
MA 132,v,172,a,108
EX 131
MA 20,a,164,v,180
CV 180,128,129
CV 181,130,131
MA 20,a,172,v,192
MA 20,a,180,v,196
RD v,204,239,4096
RD v,208,240,4096
MA 20,a,188,v,200
RD v,212,241,4096
RD a,88,247,6144
MC 136,v,144,a,112,-4.0
EX 132
MA 136,v,148,a,116
EX 133
MA 136,v,152,a,120
EX 134
MA 136,v,156,a,124
EX 135
MC 140,v,160,a,112,-4.0
CV 182,132,133
CV 183,134,135
MA 140,v,164,a,116
WR 238,180,20480
MA 140,v,168,a,120
FM 3,253,252
FM 4,253,252
MA 140,v,172,a,124
FM 5,253,252
FM 6,253,252
MA 24,a,160,v,188
EX 136
v_smfmac_f32_16x16x64_bf16 v[216:219], v[220:223], v[176:183], v234
s_waitcnt vmcnt(19) lgkmcnt(2)
s_barrier
MA 24,a,168,v,204
EX 137
MA 24,a,176,v,208
EX 138
MA 24,a,184,v,212
EX 139
MA 28,a,164,v,188
CV 188,136,137
CV 189,138,139
MA 28,a,172,v,204
RD v,192,239,16384
RD v,196,240,16384
MA 28,a,180,v,208
s_add_u32 m0, s61, 0
RD v,200,241,16384
RD a,92,247,6656
MA 28,a,188,v,212
buffer_load_dwordx4 v237, s[36:39], s60 offen offset:0 lds
s_mov_b32 s72, 20
s_cmp_eq_u32 s54, 1
s_cbranch_scc1 VER1_%=
s_cmp_eq_u32 s54, 2
s_cbranch_scc1 VER2_%=
s_cmp_eq_u32 s54, 3
s_cbranch_scc1 VER3_%=
s_branch VER0_%=
.p2align 8
VER0_%=:
.p2align 6
LOOP0_%=:
MC 128,a,0,a,64,-4.0
s_add_u32 s62, s62, 0x2000
EX 140
MA 128,a,4,a,68
EX 141
MA 128,a,8,a,72
EX 142
MA 128,a,12,a,76
EX 143
MA 16,a,192,v,176
s_waitcnt lgkmcnt(2)
CV 190,140,141
CV 191,142,143
MA 16,a,200,v,192
WR 238,188,4096
MA 16,a,208,v,196
s_waitcnt lgkmcnt(1)
FM 7,253,252
CV 32,0,1
MA 16,a,216,v,200
CV 33,2,3
CV 34,4,5
MC 132,a,16,a,64,-4.0
EX 128
MA 132,a,20,a,68
EX 129
MA 132,a,24,a,72
EX 130
MA 132,a,28,a,76
EX 131
MA 20,a,196,v,176
s_add_u32 m0, s61, 0x780
CV 176,128,129
CV 177,130,131
MA 20,a,204,v,192
buffer_load_dwordx4 v237, s[36:39], s60 offen offset:128 lds
MA 20,a,212,v,196
RD v,204,239,0
RD v,208,240,0
MA 20,a,220,v,200
RD v,212,241,0
RD a,96,247,8192
MC 136,a,0,a,80,-4.0
s_add_u32 s55, s61, 0x2000
EX 132
MA 136,a,4,a,84
s_cmp_lt_u32 s55, s66
EX 133
MA 136,a,8,a,88
s_cselect_b32 s61, s55, s64
EX 134
MA 136,a,12,a,92
s_add_u32 s60, s60, 0x2000
EX 135
MC 140,a,16,a,80,-4.0
CV 178,132,133
CV 179,134,135
MA 140,a,20,a,84
WR 238,176,8192
MA 140,a,24,a,88
CV 35,6,7
v_mul_f32 v255, -v40, v254
MA 140,a,28,a,92
FM 8,254,255
FM 9,254,255
v_smfmac_f32_16x16x64_bf16 v[216:219], v[224:227], v[184:191], v234
s_waitcnt lgkmcnt(2)
s_barrier
MA 24,a,192,v,184
EX 136
MA 24,a,200,v,204
EX 137
MA 24,a,208,v,208
EX 138
MA 24,a,216,v,212
EX 139
MA 28,a,196,v,184
CV 184,136,137
CV 185,138,139
MA 28,a,204,v,204
RD v,192,239,20480
RD v,196,240,20480
MA 28,a,212,v,208
RD v,200,241,20480
RD a,100,247,8704
MA 28,a,220,v,212
v_add_f32 v235, v216, v217
v_mov_b64 v[216:217], 0
MC 128,a,32,a,64,-4.0
EX 140
MA 128,a,36,a,68
EX 141
MA 128,a,40,a,72
EX 142
MA 128,a,44,a,76
EX 143
MA 16,a,224,v,180
s_waitcnt lgkmcnt(2)
CV 186,140,141
CV 187,142,143
MA 16,a,232,v,192
WR 238,184,0
MA 16,a,240,v,196
s_waitcnt lgkmcnt(2)
FM 10,254,255
FM 11,254,255
MA 16,a,248,v,200
FM 12,254,255
FM 13,254,255
MC 132,a,48,a,64,-4.0
EX 128
MA 132,a,52,a,68
EX 129
MA 132,a,56,a,72
EX 130
MA 132,a,60,a,76
EX 131
MA 20,a,228,v,180
CV 180,128,129
CV 181,130,131
MA 20,a,236,v,192
ds_write_b32 v242, v235 offset:512
MA 20,a,244,v,196
RD v,204,239,4096
RD v,208,240,4096
MA 20,a,252,v,200
RD v,212,241,4096
RD a,104,247,10240
MC 136,a,32,a,80,-4.0
EX 132
MA 136,a,36,a,84
EX 133
MA 136,a,40,a,88
EX 134
MA 136,a,44,a,92
EX 135
MC 140,a,48,a,80,-4.0
CV 182,132,133
CV 183,134,135
MA 140,a,52,a,84
WR 238,180,12288
MA 140,a,56,a,88
FM 14,254,255
FM 15,254,255
MA 140,a,60,a,92
CV 36,8,9
CV 37,10,11
v_smfmac_f32_16x16x64_bf16 v[216:219], v[220:223], v[176:183], v234
s_waitcnt lgkmcnt(2)
s_barrier
MA 24,a,224,v,188
EX 136
MA 24,a,232,v,204
EX 137
MA 24,a,240,v,208
EX 138
MA 24,a,248,v,212
EX 139
MA 28,a,228,v,188
CV 188,136,137
CV 189,138,139
MA 28,a,236,v,204
RD v,192,239,8192
RD v,196,240,8192
MA 28,a,244,v,208
RD v,200,241,8192
RD a,108,247,10752
MA 28,a,252,v,212
RD v,252,243,512
CV 38,12,13
MC 128,v,96,a,64,-4.0
EX 140
MA 128,v,100,a,68
EX 141
MA 128,v,104,a,72
EX 142
MA 128,v,108,a,76
EX 143
MR 0,a,128,v,176,40
s_waitcnt lgkmcnt(3)
CV 190,140,141
CV 191,142,143
MA 0,a,136,v,192
WR 238,188,4096
MA 0,a,144,v,196
s_waitcnt lgkmcnt(1)
v_add_f32 v252, v252, v253
v_add_f32 v254, v254, v255
MA 0,a,152,v,200
CV 39,14,15
v_add_f32 v45, v252, v254
MC 132,v,112,a,64,-4.0
EX 128
MA 132,v,116,a,68
EX 129
MA 132,v,120,a,72
EX 130
MA 132,v,124,a,76
EX 131
MR 4,a,132,v,176,40
CV 176,128,129
CV 177,130,131
MA 4,a,140,v,192
buffer_store_dwordx4 v[32:35], v244, s[48:51], s62 offen offset:0 sc1 nt
MA 4,a,148,v,196
RD v,204,239,0
RD v,208,240,0
MA 4,a,156,v,200
RD v,212,241,0
RD a,112,247,12288
MC 136,v,96,a,80,-4.0
EX 132
MA 136,v,100,a,84
EX 133
MA 136,v,104,a,88
EX 134
MA 136,v,108,a,92
EX 135
MC 140,v,112,a,80,-4.0
CV 178,132,133
CV 179,134,135
MA 140,v,116,a,84
WR 238,176,16384
MA 140,v,120,a,88
v_rcp_f32 v253, v45
MA 140,v,124,a,92
v_minimum3_f32 v46, v46, v44, v45
v_mov_b32 v254, v253
v_smfmac_f32_16x16x64_bf16 v[216:219], v[224:227], v[184:191], v234
s_waitcnt lgkmcnt(2)
s_barrier
MR 8,a,128,v,184,40
EX 136
MA 8,a,136,v,204
EX 137
MA 8,a,144,v,208
EX 138
MA 8,a,152,v,212
EX 139
MR 12,a,132,v,184,40
CV 184,136,137
CV 185,138,139
MA 12,a,140,v,204
RD v,192,239,12288
RD v,196,240,12288
MA 12,a,148,v,208
RD v,200,241,12288
RD a,116,247,12800
MA 12,a,156,v,212
v_permlane32_swap_b32 v253, v254
MC 128,v,144,a,64,-4.0
EX 140
MA 128,v,148,a,68
EX 141
MA 128,v,152,a,72
EX 142
MA 128,v,156,a,76
EX 143
MA 0,a,160,v,180
s_waitcnt lgkmcnt(2)
CV 186,140,141
CV 187,142,143
v_mul_f32 v252, -v40, v253
MA 0,a,168,v,192
WR 238,184,0
MA 0,a,176,v,196
s_waitcnt lgkmcnt(2)
FM 16,253,252
FM 17,253,252
MA 0,a,184,v,200
FM 18,253,252
FM 19,253,252
MC 132,v,160,a,64,-4.0
EX 128
MA 132,v,164,a,68
EX 129
MA 132,v,168,a,72
EX 130
MA 132,v,172,a,76
EX 131
MA 4,a,164,v,180
CV 180,128,129
CV 181,130,131
MA 4,a,172,v,192
buffer_store_dwordx4 v[36:39], v245, s[48:51], s62 offen offset:0 sc1 nt
MA 4,a,180,v,196
RD v,204,239,4096
RD v,208,240,4096
MA 4,a,188,v,200
RD v,212,241,4096
RD a,120,247,14336
MC 136,v,144,a,80,-4.0
EX 132
MA 136,v,148,a,84
EX 133
MA 136,v,152,a,88
)"
R"(EX 134
MA 136,v,156,a,92
EX 135
MC 140,v,160,a,80,-4.0
CV 182,132,133
CV 183,134,135
MA 140,v,164,a,84
WR 238,180,20480
MA 140,v,168,a,88
FM 20,253,252
FM 21,253,252
MA 140,v,172,a,92
FM 22,253,252
FM 23,253,252
v_smfmac_f32_16x16x64_bf16 v[216:219], v[220:223], v[176:183], v234
s_waitcnt vmcnt(12) lgkmcnt(2)
s_barrier
MA 8,a,160,v,188
EX 136
MA 8,a,168,v,204
EX 137
MA 8,a,176,v,208
EX 138
MA 8,a,184,v,212
EX 139
MA 12,a,164,v,188
CV 188,136,137
CV 189,138,139
MA 12,a,172,v,204
RD v,192,239,16384
RD v,196,240,16384
MA 12,a,180,v,208
s_add_u32 m0, s61, 0
RD v,200,241,16384
RD a,124,247,14848
MA 12,a,188,v,212
buffer_load_dwordx4 v237, s[36:39], s60 offen offset:0 lds
MC 128,a,0,a,96,-4.0
s_add_u32 s62, s62, 0x2000
EX 140
MA 128,a,4,a,100
EX 141
MA 128,a,8,a,104
EX 142
MA 128,a,12,a,108
EX 143
MA 0,a,192,v,176
s_waitcnt lgkmcnt(2)
CV 190,140,141
CV 191,142,143
MA 0,a,200,v,192
WR 238,188,4096
MA 0,a,208,v,196
s_waitcnt lgkmcnt(1)
v_add_u32 v247, s63, v236
CV 32,16,17
MA 0,a,216,v,200
CV 33,18,19
CV 34,20,21
MC 132,a,16,a,96,-4.0
s_add_u32 s55, s63, 0x4000
EX 128
MA 132,a,20,a,100
s_cmp_lt_u32 s55, 0x18000
EX 129
MA 132,a,24,a,104
s_cselect_b32 s63, s55, 0
EX 130
MA 132,a,28,a,108
EX 131
MA 4,a,196,v,176
s_add_u32 m0, s61, 0x780
CV 176,128,129
CV 177,130,131
MA 4,a,204,v,192
buffer_load_dwordx4 v237, s[36:39], s60 offen offset:128 lds
MA 4,a,212,v,196
RD v,204,239,0
RD v,208,240,0
MA 4,a,220,v,200
RD v,212,241,0
RD a,64,247,0
MC 136,a,0,a,112,-4.0
s_add_u32 s55, s61, 0x2000
EX 132
MA 136,a,4,a,116
s_cmp_lt_u32 s55, s66
EX 133
MA 136,a,8,a,120
s_cselect_b32 s61, s55, s64
EX 134
MA 136,a,12,a,124
s_add_u32 s60, s60, 0x2000
EX 135
MC 140,a,16,a,112,-4.0
CV 178,132,133
CV 179,134,135
MA 140,a,20,a,116
WR 238,176,8192
MA 140,a,24,a,120
CV 35,22,23
v_mul_f32 v255, -v40, v254
MA 140,a,28,a,124
FM 24,254,255
FM 25,254,255
v_smfmac_f32_16x16x64_bf16 v[216:219], v[224:227], v[184:191], v234
s_waitcnt lgkmcnt(2)
s_barrier
MA 8,a,192,v,184
EX 136
MA 8,a,200,v,204
EX 137
MA 8,a,208,v,208
EX 138
MA 8,a,216,v,212
EX 139
MA 12,a,196,v,184
CV 184,136,137
CV 185,138,139
MA 12,a,204,v,204
RD v,192,239,20480
RD v,196,240,20480
MA 12,a,212,v,208
RD v,200,241,20480
RD a,68,247,512
MA 12,a,220,v,212
v_add_f32 v235, v216, v217
v_mov_b64 v[216:217], 0
MC 128,a,32,a,96,-4.0
EX 140
MA 128,a,36,a,100
EX 141
MA 128,a,40,a,104
EX 142
MA 128,a,44,a,108
EX 143
MA 0,a,224,v,180
s_waitcnt lgkmcnt(2)
CV 186,140,141
CV 187,142,143
MA 0,a,232,v,192
WR 238,184,0
MA 0,a,240,v,196
s_waitcnt lgkmcnt(2)
FM 26,254,255
FM 27,254,255
MA 0,a,248,v,200
FM 28,254,255
FM 29,254,255
MC 132,a,48,a,96,-4.0
EX 128
MA 132,a,52,a,100
EX 129
MA 132,a,56,a,104
EX 130
MA 132,a,60,a,108
EX 131
MA 4,a,228,v,180
CV 180,128,129
CV 181,130,131
MA 4,a,236,v,192
ds_write_b32 v242, v235 offset:0
MA 4,a,244,v,196
RD v,204,239,4096
RD v,208,240,4096
MA 4,a,252,v,200
RD v,212,241,4096
RD a,72,247,2048
MC 136,a,32,a,112,-4.0
EX 132
MA 136,a,36,a,116
EX 133
MA 136,a,40,a,120
EX 134
MA 136,a,44,a,124
EX 135
MC 140,a,48,a,112,-4.0
CV 182,132,133
CV 183,134,135
MA 140,a,52,a,116
WR 238,180,12288
MA 140,a,56,a,120
FM 30,254,255
FM 31,254,255
MA 140,a,60,a,124
CV 36,24,25
CV 37,26,27
v_smfmac_f32_16x16x64_bf16 v[216:219], v[220:223], v[176:183], v234
s_waitcnt lgkmcnt(2)
s_barrier
MA 8,a,224,v,188
EX 136
MA 8,a,232,v,204
EX 137
MA 8,a,240,v,208
EX 138
MA 8,a,248,v,212
EX 139
MA 12,a,228,v,188
CV 188,136,137
CV 189,138,139
MA 12,a,236,v,204
RD v,192,239,8192
RD v,196,240,8192
MA 12,a,244,v,208
RD v,200,241,8192
RD a,76,247,2560
MA 12,a,252,v,212
RD v,252,243,0
CV 38,28,29
MC 128,v,96,a,96,-4.0
EX 140
MA 128,v,100,a,100
EX 141
MA 128,v,104,a,104
EX 142
MA 128,v,108,a,108
EX 143
MR 16,a,128,v,176,40
s_waitcnt lgkmcnt(3)
CV 190,140,141
CV 191,142,143
MA 16,a,136,v,192
WR 238,188,4096
MA 16,a,144,v,196
s_waitcnt lgkmcnt(1)
v_add_f32 v252, v252, v253
v_add_f32 v254, v254, v255
MA 16,a,152,v,200
CV 39,30,31
v_add_f32 v44, v252, v254
MC 132,v,112,a,96,-4.0
EX 128
MA 132,v,116,a,100
EX 129
MA 132,v,120,a,104
EX 130
MA 132,v,124,a,108
EX 131
MR 20,a,132,v,176,40
CV 176,128,129
CV 177,130,131
MA 20,a,140,v,192
buffer_store_dwordx4 v[32:35], v244, s[48:51], s62 offen offset:0 sc1 nt
MA 20,a,148,v,196
RD v,204,239,0
RD v,208,240,0
MA 20,a,156,v,200
RD v,212,241,0
RD a,80,247,4096
MC 136,v,96,a,112,-4.0
EX 132
MA 136,v,100,a,116
EX 133
MA 136,v,104,a,120
EX 134
MA 136,v,108,a,124
EX 135
MC 140,v,112,a,112,-4.0
CV 178,132,133
CV 179,134,135
MA 140,v,116,a,116
WR 238,176,16384
MA 140,v,120,a,120
v_rcp_f32 v253, v44
MA 140,v,124,a,124
v_maximum3_f32 v246, v246, v44, v45
v_mov_b32 v254, v253
v_smfmac_f32_16x16x64_bf16 v[216:219], v[224:227], v[184:191], v234
s_waitcnt lgkmcnt(2)
s_barrier
MR 24,a,128,v,184,40
EX 136
MA 24,a,136,v,204
EX 137
MA 24,a,144,v,208
EX 138
MA 24,a,152,v,212
EX 139
MR 28,a,132,v,184,40
CV 184,136,137
CV 185,138,139
MA 28,a,140,v,204
RD v,192,239,12288
RD v,196,240,12288
MA 28,a,148,v,208
RD v,200,241,12288
RD a,84,247,4608
MA 28,a,156,v,212
v_permlane32_swap_b32 v253, v254
MC 128,v,144,a,96,-4.0
EX 140
MA 128,v,148,a,100
EX 141
MA 128,v,152,a,104
EX 142
MA 128,v,156,a,108
EX 143
MA 16,a,160,v,180
s_waitcnt lgkmcnt(2)
CV 186,140,141
CV 187,142,143
MA 16,a,168,v,192
WR 238,184,0
MA 16,a,176,v,196
s_waitcnt lgkmcnt(2)
v_mul_f32 v252, -v40, v253
FM 0,253,252
MA 16,a,184,v,200
FM 1,253,252
FM 2,253,252
MC 132,v,160,a,96,-4.0
EX 128
MA 132,v,164,a,100
EX 129
MA 132,v,168,a,104
EX 130
MA 132,v,172,a,108
EX 131
MA 20,a,164,v,180
CV 180,128,129
CV 181,130,131
MA 20,a,172,v,192
buffer_store_dwordx4 v[36:39], v245, s[48:51], s62 offen offset:0 sc1 nt
MA 20,a,180,v,196
RD v,204,239,4096
RD v,208,240,4096
MA 20,a,188,v,200
RD v,212,241,4096
RD a,88,247,6144
MC 136,v,144,a,112,-4.0
EX 132
MA 136,v,148,a,116
EX 133
MA 136,v,152,a,120
EX 134
MA 136,v,156,a,124
EX 135
MC 140,v,160,a,112,-4.0
CV 182,132,133
CV 183,134,135
MA 140,v,164,a,116
WR 238,180,20480
MA 140,v,168,a,120
FM 3,253,252
FM 4,253,252
MA 140,v,172,a,124
FM 5,253,252
FM 6,253,252
v_smfmac_f32_16x16x64_bf16 v[216:219], v[220:223], v[176:183], v234
s_waitcnt vmcnt(14) lgkmcnt(2)
s_barrier
MA 24,a,160,v,188
EX 136
MA 24,a,168,v,204
EX 137
MA 24,a,176,v,208
EX 138
MA 24,a,184,v,212
EX 139
MA 28,a,164,v,188
CV 188,136,137
CV 189,138,139
MA 28,a,172,v,204
RD v,192,239,16384
RD v,196,240,16384
MA 28,a,180,v,208
s_add_u32 m0, s61, 0
RD v,200,241,16384
RD a,92,247,6656
MA 28,a,188,v,212
s_add_u32 s72, s72, -1
buffer_load_dwordx4 v237, s[36:39], s60 offen offset:0 lds
s_cbranch_scc1 LOOP0_%=
s_branch TAIL%=
.p2align 8
VER1_%=:
.p2align 6
LOOP1_%=:
MC 128,a,0,a,64,-4.0
s_add_u32 s62, s62, 0x2000
EX 140
MA 128,a,4,a,68
EX 141
MA 128,a,8,a,72
EX 142
MA 128,a,12,a,76
EX 143
MA 16,a,192,v,176
s_waitcnt lgkmcnt(2)
CV 190,140,141
CV 191,142,143
MA 16,a,200,v,192
WR 238,188,4096
MA 16,a,208,v,196
s_waitcnt lgkmcnt(1)
FM 7,253,252
CV 32,0,1
MA 16,a,216,v,200
CV 33,2,3
)"
R"(CV 34,4,5
MC 132,a,16,a,64,-4.0
EX 128
MA 132,a,20,a,68
EX 129
MA 132,a,24,a,72
EX 130
MA 132,a,28,a,76
EX 131
MA 20,a,196,v,176
s_add_u32 m0, s61, 0x780
CV 176,128,129
CV 177,130,131
MA 20,a,204,v,192
buffer_load_dwordx4 v237, s[36:39], s60 offen offset:128 lds
MA 20,a,212,v,196
RD v,204,239,0
RD v,208,240,0
MA 20,a,220,v,200
RD v,212,241,0
RD a,96,247,8192
MC 136,a,0,a,80,-4.0
s_add_u32 s55, s61, 0x2000
EX 132
MA 136,a,4,a,84
s_cmp_lt_u32 s55, s66
EX 133
MA 136,a,8,a,88
s_cselect_b32 s61, s55, s64
EX 134
MA 136,a,12,a,92
s_add_u32 s60, s60, 0x2000
EX 135
MC 140,a,16,a,80,-4.0
CV 178,132,133
CV 179,134,135
MA 140,a,20,a,84
WR 238,176,8192
MA 140,a,24,a,88
CV 35,6,7
v_mul_f32 v255, -v40, v254
MA 140,a,28,a,92
FM 8,254,255
FM 9,254,255
MA 24,a,192,v,184
EX 136
v_smfmac_f32_16x16x64_bf16 v[216:219], v[224:227], v[184:191], v234
s_waitcnt lgkmcnt(2)
s_barrier
MA 24,a,200,v,204
EX 137
MA 24,a,208,v,208
EX 138
MA 24,a,216,v,212
EX 139
MA 28,a,196,v,184
CV 184,136,137
CV 185,138,139
MA 28,a,204,v,204
RD v,192,239,20480
RD v,196,240,20480
MA 28,a,212,v,208
RD v,200,241,20480
RD a,100,247,8704
MA 28,a,220,v,212
v_add_f32 v235, v216, v217
v_mov_b64 v[216:217], 0
MC 128,a,32,a,64,-4.0
EX 140
MA 128,a,36,a,68
EX 141
MA 128,a,40,a,72
EX 142
MA 128,a,44,a,76
EX 143
MA 16,a,224,v,180
s_waitcnt lgkmcnt(2)
CV 186,140,141
CV 187,142,143
MA 16,a,232,v,192
WR 238,184,0
MA 16,a,240,v,196
s_waitcnt lgkmcnt(2)
FM 10,254,255
FM 11,254,255
MA 16,a,248,v,200
FM 12,254,255
FM 13,254,255
MC 132,a,48,a,64,-4.0
EX 128
MA 132,a,52,a,68
EX 129
MA 132,a,56,a,72
EX 130
MA 132,a,60,a,76
EX 131
MA 20,a,228,v,180
CV 180,128,129
CV 181,130,131
MA 20,a,236,v,192
ds_write_b32 v242, v235 offset:512
MA 20,a,244,v,196
RD v,204,239,4096
RD v,208,240,4096
MA 20,a,252,v,200
RD v,212,241,4096
RD a,104,247,10240
MC 136,a,32,a,80,-4.0
EX 132
MA 136,a,36,a,84
EX 133
MA 136,a,40,a,88
EX 134
MA 136,a,44,a,92
EX 135
MC 140,a,48,a,80,-4.0
CV 182,132,133
CV 183,134,135
MA 140,a,52,a,84
WR 238,180,12288
MA 140,a,56,a,88
FM 14,254,255
FM 15,254,255
MA 140,a,60,a,92
CV 36,8,9
CV 37,10,11
MA 24,a,224,v,188
EX 136
v_smfmac_f32_16x16x64_bf16 v[216:219], v[220:223], v[176:183], v234
s_waitcnt lgkmcnt(2)
s_barrier
MA 24,a,232,v,204
EX 137
MA 24,a,240,v,208
EX 138
MA 24,a,248,v,212
EX 139
MA 28,a,228,v,188
CV 188,136,137
CV 189,138,139
MA 28,a,236,v,204
RD v,192,239,8192
RD v,196,240,8192
MA 28,a,244,v,208
RD v,200,241,8192
RD a,108,247,10752
MA 28,a,252,v,212
RD v,252,243,512
CV 38,12,13
MC 128,v,96,a,64,-4.0
EX 140
MA 128,v,100,a,68
EX 141
MA 128,v,104,a,72
EX 142
MA 128,v,108,a,76
EX 143
MR 0,a,128,v,176,40
s_waitcnt lgkmcnt(3)
CV 190,140,141
CV 191,142,143
MA 0,a,136,v,192
WR 238,188,4096
MA 0,a,144,v,196
s_waitcnt lgkmcnt(1)
v_add_f32 v252, v252, v253
v_add_f32 v254, v254, v255
MA 0,a,152,v,200
CV 39,14,15
v_add_f32 v45, v252, v254
MC 132,v,112,a,64,-4.0
EX 128
MA 132,v,116,a,68
EX 129
MA 132,v,120,a,72
EX 130
MA 132,v,124,a,76
EX 131
MR 4,a,132,v,176,40
CV 176,128,129
CV 177,130,131
MA 4,a,140,v,192
buffer_store_dwordx4 v[32:35], v244, s[48:51], s62 offen offset:0 sc1 nt
MA 4,a,148,v,196
RD v,204,239,0
RD v,208,240,0
MA 4,a,156,v,200
RD v,212,241,0
RD a,112,247,12288
MC 136,v,96,a,80,-4.0
EX 132
MA 136,v,100,a,84
EX 133
MA 136,v,104,a,88
EX 134
MA 136,v,108,a,92
EX 135
MC 140,v,112,a,80,-4.0
CV 178,132,133
CV 179,134,135
MA 140,v,116,a,84
WR 238,176,16384
MA 140,v,120,a,88
v_rcp_f32 v253, v45
MA 140,v,124,a,92
v_minimum3_f32 v46, v46, v44, v45
v_mov_b32 v254, v253
MR 8,a,128,v,184,40
EX 136
v_smfmac_f32_16x16x64_bf16 v[216:219], v[224:227], v[184:191], v234
s_waitcnt lgkmcnt(2)
s_barrier
MA 8,a,136,v,204
EX 137
MA 8,a,144,v,208
EX 138
MA 8,a,152,v,212
EX 139
MR 12,a,132,v,184,40
CV 184,136,137
CV 185,138,139
MA 12,a,140,v,204
RD v,192,239,12288
RD v,196,240,12288
MA 12,a,148,v,208
RD v,200,241,12288
RD a,116,247,12800
MA 12,a,156,v,212
v_permlane32_swap_b32 v253, v254
MC 128,v,144,a,64,-4.0
EX 140
MA 128,v,148,a,68
EX 141
MA 128,v,152,a,72
EX 142
MA 128,v,156,a,76
EX 143
MA 0,a,160,v,180
s_waitcnt lgkmcnt(2)
CV 186,140,141
CV 187,142,143
v_mul_f32 v252, -v40, v253
MA 0,a,168,v,192
WR 238,184,0
MA 0,a,176,v,196
s_waitcnt lgkmcnt(2)
FM 16,253,252
FM 17,253,252
MA 0,a,184,v,200
FM 18,253,252
FM 19,253,252
MC 132,v,160,a,64,-4.0
EX 128
MA 132,v,164,a,68
EX 129
MA 132,v,168,a,72
EX 130
MA 132,v,172,a,76
EX 131
MA 4,a,164,v,180
CV 180,128,129
CV 181,130,131
MA 4,a,172,v,192
buffer_store_dwordx4 v[36:39], v245, s[48:51], s62 offen offset:0 sc1 nt
MA 4,a,180,v,196
RD v,204,239,4096
RD v,208,240,4096
MA 4,a,188,v,200
RD v,212,241,4096
RD a,120,247,14336
MC 136,v,144,a,80,-4.0
EX 132
MA 136,v,148,a,84
EX 133
MA 136,v,152,a,88
EX 134
MA 136,v,156,a,92
EX 135
MC 140,v,160,a,80,-4.0
CV 182,132,133
CV 183,134,135
MA 140,v,164,a,84
WR 238,180,20480
MA 140,v,168,a,88
FM 20,253,252
FM 21,253,252
MA 140,v,172,a,92
FM 22,253,252
FM 23,253,252
MA 8,a,160,v,188
EX 136
v_smfmac_f32_16x16x64_bf16 v[216:219], v[220:223], v[176:183], v234
s_waitcnt vmcnt(12) lgkmcnt(2)
s_barrier
MA 8,a,168,v,204
EX 137
MA 8,a,176,v,208
EX 138
MA 8,a,184,v,212
EX 139
MA 12,a,164,v,188
CV 188,136,137
CV 189,138,139
MA 12,a,172,v,204
RD v,192,239,16384
RD v,196,240,16384
MA 12,a,180,v,208
s_add_u32 m0, s61, 0
RD v,200,241,16384
RD a,124,247,14848
MA 12,a,188,v,212
buffer_load_dwordx4 v237, s[36:39], s60 offen offset:0 lds
MC 128,a,0,a,96,-4.0
s_add_u32 s62, s62, 0x2000
EX 140
MA 128,a,4,a,100
EX 141
MA 128,a,8,a,104
EX 142
MA 128,a,12,a,108
EX 143
MA 0,a,192,v,176
s_waitcnt lgkmcnt(2)
CV 190,140,141
CV 191,142,143
MA 0,a,200,v,192
WR 238,188,4096
MA 0,a,208,v,196
s_waitcnt lgkmcnt(1)
v_add_u32 v247, s63, v236
CV 32,16,17
MA 0,a,216,v,200
CV 33,18,19
CV 34,20,21
MC 132,a,16,a,96,-4.0
s_add_u32 s55, s63, 0x4000
EX 128
MA 132,a,20,a,100
s_cmp_lt_u32 s55, 0x18000
EX 129
MA 132,a,24,a,104
s_cselect_b32 s63, s55, 0
EX 130
MA 132,a,28,a,108
EX 131
MA 4,a,196,v,176
s_add_u32 m0, s61, 0x780
CV 176,128,129
CV 177,130,131
MA 4,a,204,v,192
buffer_load_dwordx4 v237, s[36:39], s60 offen offset:128 lds
MA 4,a,212,v,196
RD v,204,239,0
RD v,208,240,0
MA 4,a,220,v,200
RD v,212,241,0
RD a,64,247,0
MC 136,a,0,a,112,-4.0
s_add_u32 s55, s61, 0x2000
EX 132
MA 136,a,4,a,116
s_cmp_lt_u32 s55, s66
EX 133
MA 136,a,8,a,120
s_cselect_b32 s61, s55, s64
EX 134
MA 136,a,12,a,124
s_add_u32 s60, s60, 0x2000
EX 135
MC 140,a,16,a,112,-4.0
CV 178,132,133
CV 179,134,135
MA 140,a,20,a,116
WR 238,176,8192
MA 140,a,24,a,120
CV 35,22,23
v_mul_f32 v255, -v40, v254
MA 140,a,28,a,124
FM 24,254,255
FM 25,254,255
MA 8,a,192,v,184
EX 136
v_smfmac_f32_16x16x64_bf16 v[216:219], v[224:227], v[184:191], v234
s_waitcnt lgkmcnt(2)
s_barrier
MA 8,a,200,v,204
EX 137
MA 8,a,208,v,208
EX 138
MA 8,a,216,v,212
EX 139
MA 12,a,196,v,184
CV 184,136,137
CV 185,138,139
MA 12,a,204,v,204
RD v,192,239,20480
RD v,196,240,20480
MA 12,a,212,v,208
RD v,200,241,20480
RD a,68,247,512
)"
R"(MA 12,a,220,v,212
v_add_f32 v235, v216, v217
v_mov_b64 v[216:217], 0
MC 128,a,32,a,96,-4.0
EX 140
MA 128,a,36,a,100
EX 141
MA 128,a,40,a,104
EX 142
MA 128,a,44,a,108
EX 143
MA 0,a,224,v,180
s_waitcnt lgkmcnt(2)
CV 186,140,141
CV 187,142,143
MA 0,a,232,v,192
WR 238,184,0
MA 0,a,240,v,196
s_waitcnt lgkmcnt(2)
FM 26,254,255
FM 27,254,255
MA 0,a,248,v,200
FM 28,254,255
FM 29,254,255
MC 132,a,48,a,96,-4.0
EX 128
MA 132,a,52,a,100
EX 129
MA 132,a,56,a,104
EX 130
MA 132,a,60,a,108
EX 131
MA 4,a,228,v,180
CV 180,128,129
CV 181,130,131
MA 4,a,236,v,192
ds_write_b32 v242, v235 offset:0
MA 4,a,244,v,196
RD v,204,239,4096
RD v,208,240,4096
MA 4,a,252,v,200
RD v,212,241,4096
RD a,72,247,2048
MC 136,a,32,a,112,-4.0
EX 132
MA 136,a,36,a,116
EX 133
MA 136,a,40,a,120
EX 134
MA 136,a,44,a,124
EX 135
MC 140,a,48,a,112,-4.0
CV 182,132,133
CV 183,134,135
MA 140,a,52,a,116
WR 238,180,12288
MA 140,a,56,a,120
FM 30,254,255
FM 31,254,255
MA 140,a,60,a,124
CV 36,24,25
CV 37,26,27
MA 8,a,224,v,188
EX 136
v_smfmac_f32_16x16x64_bf16 v[216:219], v[220:223], v[176:183], v234
s_waitcnt lgkmcnt(2)
s_barrier
MA 8,a,232,v,204
EX 137
MA 8,a,240,v,208
EX 138
MA 8,a,248,v,212
EX 139
MA 12,a,228,v,188
CV 188,136,137
CV 189,138,139
MA 12,a,236,v,204
RD v,192,239,8192
RD v,196,240,8192
MA 12,a,244,v,208
RD v,200,241,8192
RD a,76,247,2560
MA 12,a,252,v,212
RD v,252,243,0
CV 38,28,29
MC 128,v,96,a,96,-4.0
EX 140
MA 128,v,100,a,100
EX 141
MA 128,v,104,a,104
EX 142
MA 128,v,108,a,108
EX 143
MR 16,a,128,v,176,40
s_waitcnt lgkmcnt(3)
CV 190,140,141
CV 191,142,143
MA 16,a,136,v,192
WR 238,188,4096
MA 16,a,144,v,196
s_waitcnt lgkmcnt(1)
v_add_f32 v252, v252, v253
v_add_f32 v254, v254, v255
MA 16,a,152,v,200
CV 39,30,31
v_add_f32 v44, v252, v254
MC 132,v,112,a,96,-4.0
EX 128
MA 132,v,116,a,100
EX 129
MA 132,v,120,a,104
EX 130
MA 132,v,124,a,108
EX 131
MR 20,a,132,v,176,40
CV 176,128,129
CV 177,130,131
MA 20,a,140,v,192
buffer_store_dwordx4 v[32:35], v244, s[48:51], s62 offen offset:0 sc1 nt
MA 20,a,148,v,196
RD v,204,239,0
RD v,208,240,0
MA 20,a,156,v,200
RD v,212,241,0
RD a,80,247,4096
MC 136,v,96,a,112,-4.0
EX 132
MA 136,v,100,a,116
EX 133
MA 136,v,104,a,120
EX 134
MA 136,v,108,a,124
EX 135
MC 140,v,112,a,112,-4.0
CV 178,132,133
CV 179,134,135
MA 140,v,116,a,116
WR 238,176,16384
MA 140,v,120,a,120
v_rcp_f32 v253, v44
MA 140,v,124,a,124
v_maximum3_f32 v246, v246, v44, v45
v_mov_b32 v254, v253
MR 24,a,128,v,184,40
EX 136
v_smfmac_f32_16x16x64_bf16 v[216:219], v[224:227], v[184:191], v234
s_waitcnt lgkmcnt(2)
s_barrier
MA 24,a,136,v,204
EX 137
MA 24,a,144,v,208
EX 138
MA 24,a,152,v,212
EX 139
MR 28,a,132,v,184,40
CV 184,136,137
CV 185,138,139
MA 28,a,140,v,204
RD v,192,239,12288
RD v,196,240,12288
MA 28,a,148,v,208
RD v,200,241,12288
RD a,84,247,4608
MA 28,a,156,v,212
v_permlane32_swap_b32 v253, v254
MC 128,v,144,a,96,-4.0
EX 140
MA 128,v,148,a,100
EX 141
MA 128,v,152,a,104
EX 142
MA 128,v,156,a,108
EX 143
MA 16,a,160,v,180
s_waitcnt lgkmcnt(2)
CV 186,140,141
CV 187,142,143
MA 16,a,168,v,192
WR 238,184,0
MA 16,a,176,v,196
s_waitcnt lgkmcnt(2)
v_mul_f32 v252, -v40, v253
FM 0,253,252
MA 16,a,184,v,200
FM 1,253,252
FM 2,253,252
MC 132,v,160,a,96,-4.0
EX 128
MA 132,v,164,a,100
EX 129
MA 132,v,168,a,104
EX 130
MA 132,v,172,a,108
EX 131
MA 20,a,164,v,180
CV 180,128,129
CV 181,130,131
MA 20,a,172,v,192
buffer_store_dwordx4 v[36:39], v245, s[48:51], s62 offen offset:0 sc1 nt
MA 20,a,180,v,196
RD v,204,239,4096
RD v,208,240,4096
MA 20,a,188,v,200
RD v,212,241,4096
RD a,88,247,6144
MC 136,v,144,a,112,-4.0
EX 132
MA 136,v,148,a,116
EX 133
MA 136,v,152,a,120
EX 134
MA 136,v,156,a,124
EX 135
MC 140,v,160,a,112,-4.0
CV 182,132,133
CV 183,134,135
MA 140,v,164,a,116
WR 238,180,20480
MA 140,v,168,a,120
FM 3,253,252
FM 4,253,252
MA 140,v,172,a,124
FM 5,253,252
FM 6,253,252
MA 24,a,160,v,188
EX 136
v_smfmac_f32_16x16x64_bf16 v[216:219], v[220:223], v[176:183], v234
s_waitcnt vmcnt(14) lgkmcnt(2)
s_barrier
MA 24,a,168,v,204
EX 137
MA 24,a,176,v,208
EX 138
MA 24,a,184,v,212
EX 139
MA 28,a,164,v,188
CV 188,136,137
CV 189,138,139
MA 28,a,172,v,204
RD v,192,239,16384
RD v,196,240,16384
MA 28,a,180,v,208
s_add_u32 m0, s61, 0
RD v,200,241,16384
RD a,92,247,6656
MA 28,a,188,v,212
s_add_u32 s72, s72, -1
buffer_load_dwordx4 v237, s[36:39], s60 offen offset:0 lds
s_cbranch_scc1 LOOP1_%=
s_branch TAIL%=
.p2align 8
VER2_%=:
.p2align 6
LOOP2_%=:
MC 128,a,0,a,64,-4.0
s_add_u32 s62, s62, 0x2000
EX 140
MA 128,a,4,a,68
EX 141
MA 128,a,8,a,72
EX 142
MA 128,a,12,a,76
EX 143
MA 16,a,192,v,176
s_waitcnt lgkmcnt(2)
CV 190,140,141
CV 191,142,143
MA 16,a,200,v,192
WR 238,188,4096
MA 16,a,208,v,196
s_waitcnt lgkmcnt(1)
FM 7,253,252
CV 32,0,1
MA 16,a,216,v,200
CV 33,2,3
CV 34,4,5
MC 132,a,16,a,64,-4.0
EX 128
MA 132,a,20,a,68
EX 129
MA 132,a,24,a,72
EX 130
MA 132,a,28,a,76
EX 131
MA 20,a,196,v,176
s_add_u32 m0, s61, 0x780
CV 176,128,129
CV 177,130,131
MA 20,a,204,v,192
buffer_load_dwordx4 v237, s[36:39], s60 offen offset:128 lds
MA 20,a,212,v,196
RD v,204,239,0
RD v,208,240,0
MA 20,a,220,v,200
RD v,212,241,0
RD a,96,247,8192
MC 136,a,0,a,80,-4.0
s_add_u32 s55, s61, 0x2000
EX 132
MA 136,a,4,a,84
s_cmp_lt_u32 s55, s66
EX 133
MA 136,a,8,a,88
s_cselect_b32 s61, s55, s64
EX 134
MA 136,a,12,a,92
s_add_u32 s60, s60, 0x2000
EX 135
MC 140,a,16,a,80,-4.0
CV 178,132,133
CV 179,134,135
MA 140,a,20,a,84
WR 238,176,8192
MA 140,a,24,a,88
CV 35,6,7
v_mul_f32 v255, -v40, v254
MA 140,a,28,a,92
FM 8,254,255
FM 9,254,255
MA 24,a,192,v,184
s_waitcnt lgkmcnt(2)
EX 136
MA 24,a,200,v,204
EX 137
v_smfmac_f32_16x16x64_bf16 v[216:219], v[224:227], v[184:191], v234
s_barrier
MA 24,a,208,v,208
EX 138
MA 24,a,216,v,212
EX 139
MA 28,a,196,v,184
CV 184,136,137
CV 185,138,139
MA 28,a,204,v,204
RD v,192,239,20480
RD v,196,240,20480
MA 28,a,212,v,208
RD v,200,241,20480
RD a,100,247,8704
MA 28,a,220,v,212
v_add_f32 v235, v216, v217
v_mov_b64 v[216:217], 0
MC 128,a,32,a,64,-4.0
EX 140
MA 128,a,36,a,68
EX 141
MA 128,a,40,a,72
EX 142
MA 128,a,44,a,76
EX 143
MA 16,a,224,v,180
s_waitcnt lgkmcnt(2)
CV 186,140,141
CV 187,142,143
MA 16,a,232,v,192
WR 238,184,0
MA 16,a,240,v,196
s_waitcnt lgkmcnt(2)
FM 10,254,255
FM 11,254,255
MA 16,a,248,v,200
FM 12,254,255
FM 13,254,255
MC 132,a,48,a,64,-4.0
EX 128
MA 132,a,52,a,68
EX 129
MA 132,a,56,a,72
EX 130
MA 132,a,60,a,76
EX 131
MA 20,a,228,v,180
CV 180,128,129
CV 181,130,131
MA 20,a,236,v,192
ds_write_b32 v242, v235 offset:512
MA 20,a,244,v,196
RD v,204,239,4096
RD v,208,240,4096
MA 20,a,252,v,200
RD v,212,241,4096
RD a,104,247,10240
MC 136,a,32,a,80,-4.0
EX 132
MA 136,a,36,a,84
EX 133
MA 136,a,40,a,88
EX 134
MA 136,a,44,a,92
EX 135
MC 140,a,48,a,80,-4.0
CV 182,132,133
CV 183,134,135
MA 140,a,52,a,84
WR 238,180,12288
MA 140,a,56,a,88
FM 14,254,255
FM 15,254,255
MA 140,a,60,a,92
CV 36,8,9
)"
R"(CV 37,10,11
MA 24,a,224,v,188
s_waitcnt lgkmcnt(2)
EX 136
MA 24,a,232,v,204
EX 137
v_smfmac_f32_16x16x64_bf16 v[216:219], v[220:223], v[176:183], v234
s_barrier
MA 24,a,240,v,208
EX 138
MA 24,a,248,v,212
EX 139
MA 28,a,228,v,188
CV 188,136,137
CV 189,138,139
MA 28,a,236,v,204
RD v,192,239,8192
RD v,196,240,8192
MA 28,a,244,v,208
RD v,200,241,8192
RD a,108,247,10752
MA 28,a,252,v,212
RD v,252,243,512
CV 38,12,13
MC 128,v,96,a,64,-4.0
EX 140
MA 128,v,100,a,68
EX 141
MA 128,v,104,a,72
EX 142
MA 128,v,108,a,76
EX 143
MR 0,a,128,v,176,40
s_waitcnt lgkmcnt(3)
CV 190,140,141
CV 191,142,143
MA 0,a,136,v,192
WR 238,188,4096
MA 0,a,144,v,196
s_waitcnt lgkmcnt(1)
v_add_f32 v252, v252, v253
v_add_f32 v254, v254, v255
MA 0,a,152,v,200
CV 39,14,15
v_add_f32 v45, v252, v254
MC 132,v,112,a,64,-4.0
EX 128
MA 132,v,116,a,68
EX 129
MA 132,v,120,a,72
EX 130
MA 132,v,124,a,76
EX 131
MR 4,a,132,v,176,40
CV 176,128,129
CV 177,130,131
MA 4,a,140,v,192
buffer_store_dwordx4 v[32:35], v244, s[48:51], s62 offen offset:0 sc1 nt
MA 4,a,148,v,196
RD v,204,239,0
RD v,208,240,0
MA 4,a,156,v,200
RD v,212,241,0
RD a,112,247,12288
MC 136,v,96,a,80,-4.0
EX 132
MA 136,v,100,a,84
EX 133
MA 136,v,104,a,88
EX 134
MA 136,v,108,a,92
EX 135
MC 140,v,112,a,80,-4.0
CV 178,132,133
CV 179,134,135
MA 140,v,116,a,84
WR 238,176,16384
MA 140,v,120,a,88
v_rcp_f32 v253, v45
MA 140,v,124,a,92
v_minimum3_f32 v46, v46, v44, v45
v_mov_b32 v254, v253
MR 8,a,128,v,184,40
s_waitcnt lgkmcnt(2)
EX 136
MA 8,a,136,v,204
EX 137
v_smfmac_f32_16x16x64_bf16 v[216:219], v[224:227], v[184:191], v234
s_barrier
MA 8,a,144,v,208
EX 138
MA 8,a,152,v,212
EX 139
MR 12,a,132,v,184,40
CV 184,136,137
CV 185,138,139
MA 12,a,140,v,204
RD v,192,239,12288
RD v,196,240,12288
MA 12,a,148,v,208
RD v,200,241,12288
RD a,116,247,12800
MA 12,a,156,v,212
v_permlane32_swap_b32 v253, v254
MC 128,v,144,a,64,-4.0
EX 140
MA 128,v,148,a,68
EX 141
MA 128,v,152,a,72
EX 142
MA 128,v,156,a,76
EX 143
MA 0,a,160,v,180
s_waitcnt lgkmcnt(2)
CV 186,140,141
CV 187,142,143
v_mul_f32 v252, -v40, v253
MA 0,a,168,v,192
WR 238,184,0
MA 0,a,176,v,196
s_waitcnt lgkmcnt(2)
FM 16,253,252
FM 17,253,252
MA 0,a,184,v,200
FM 18,253,252
FM 19,253,252
MC 132,v,160,a,64,-4.0
EX 128
MA 132,v,164,a,68
EX 129
MA 132,v,168,a,72
EX 130
MA 132,v,172,a,76
EX 131
MA 4,a,164,v,180
CV 180,128,129
CV 181,130,131
MA 4,a,172,v,192
buffer_store_dwordx4 v[36:39], v245, s[48:51], s62 offen offset:0 sc1 nt
MA 4,a,180,v,196
RD v,204,239,4096
RD v,208,240,4096
MA 4,a,188,v,200
RD v,212,241,4096
RD a,120,247,14336
MC 136,v,144,a,80,-4.0
EX 132
MA 136,v,148,a,84
EX 133
MA 136,v,152,a,88
EX 134
MA 136,v,156,a,92
EX 135
MC 140,v,160,a,80,-4.0
CV 182,132,133
CV 183,134,135
MA 140,v,164,a,84
WR 238,180,20480
MA 140,v,168,a,88
FM 20,253,252
FM 21,253,252
MA 140,v,172,a,92
FM 22,253,252
FM 23,253,252
MA 8,a,160,v,188
s_waitcnt lgkmcnt(2)
EX 136
MA 8,a,168,v,204
EX 137
v_smfmac_f32_16x16x64_bf16 v[216:219], v[220:223], v[176:183], v234
s_waitcnt vmcnt(12)
s_barrier
MA 8,a,176,v,208
EX 138
MA 8,a,184,v,212
EX 139
MA 12,a,164,v,188
CV 188,136,137
CV 189,138,139
MA 12,a,172,v,204
RD v,192,239,16384
RD v,196,240,16384
MA 12,a,180,v,208
s_add_u32 m0, s61, 0
RD v,200,241,16384
RD a,124,247,14848
MA 12,a,188,v,212
buffer_load_dwordx4 v237, s[36:39], s60 offen offset:0 lds
MC 128,a,0,a,96,-4.0
s_add_u32 s62, s62, 0x2000
EX 140
MA 128,a,4,a,100
EX 141
MA 128,a,8,a,104
EX 142
MA 128,a,12,a,108
EX 143
MA 0,a,192,v,176
s_waitcnt lgkmcnt(2)
CV 190,140,141
CV 191,142,143
MA 0,a,200,v,192
WR 238,188,4096
MA 0,a,208,v,196
s_waitcnt lgkmcnt(1)
v_add_u32 v247, s63, v236
CV 32,16,17
MA 0,a,216,v,200
CV 33,18,19
CV 34,20,21
MC 132,a,16,a,96,-4.0
s_add_u32 s55, s63, 0x4000
EX 128
MA 132,a,20,a,100
s_cmp_lt_u32 s55, 0x18000
EX 129
MA 132,a,24,a,104
s_cselect_b32 s63, s55, 0
EX 130
MA 132,a,28,a,108
EX 131
MA 4,a,196,v,176
s_add_u32 m0, s61, 0x780
CV 176,128,129
CV 177,130,131
MA 4,a,204,v,192
buffer_load_dwordx4 v237, s[36:39], s60 offen offset:128 lds
MA 4,a,212,v,196
RD v,204,239,0
RD v,208,240,0
MA 4,a,220,v,200
RD v,212,241,0
RD a,64,247,0
MC 136,a,0,a,112,-4.0
s_add_u32 s55, s61, 0x2000
EX 132
MA 136,a,4,a,116
s_cmp_lt_u32 s55, s66
EX 133
MA 136,a,8,a,120
s_cselect_b32 s61, s55, s64
EX 134
MA 136,a,12,a,124
s_add_u32 s60, s60, 0x2000
EX 135
MC 140,a,16,a,112,-4.0
CV 178,132,133
CV 179,134,135
MA 140,a,20,a,116
WR 238,176,8192
MA 140,a,24,a,120
CV 35,22,23
v_mul_f32 v255, -v40, v254
MA 140,a,28,a,124
FM 24,254,255
FM 25,254,255
MA 8,a,192,v,184
s_waitcnt lgkmcnt(2)
EX 136
MA 8,a,200,v,204
EX 137
v_smfmac_f32_16x16x64_bf16 v[216:219], v[224:227], v[184:191], v234
s_barrier
MA 8,a,208,v,208
EX 138
MA 8,a,216,v,212
EX 139
MA 12,a,196,v,184
CV 184,136,137
CV 185,138,139
MA 12,a,204,v,204
RD v,192,239,20480
RD v,196,240,20480
MA 12,a,212,v,208
RD v,200,241,20480
RD a,68,247,512
MA 12,a,220,v,212
v_add_f32 v235, v216, v217
v_mov_b64 v[216:217], 0
MC 128,a,32,a,96,-4.0
EX 140
MA 128,a,36,a,100
EX 141
MA 128,a,40,a,104
EX 142
MA 128,a,44,a,108
EX 143
MA 0,a,224,v,180
s_waitcnt lgkmcnt(2)
CV 186,140,141
CV 187,142,143
MA 0,a,232,v,192
WR 238,184,0
MA 0,a,240,v,196
s_waitcnt lgkmcnt(2)
FM 26,254,255
FM 27,254,255
MA 0,a,248,v,200
FM 28,254,255
FM 29,254,255
MC 132,a,48,a,96,-4.0
EX 128
MA 132,a,52,a,100
EX 129
MA 132,a,56,a,104
EX 130
MA 132,a,60,a,108
EX 131
MA 4,a,228,v,180
CV 180,128,129
CV 181,130,131
MA 4,a,236,v,192
ds_write_b32 v242, v235 offset:0
MA 4,a,244,v,196
RD v,204,239,4096
RD v,208,240,4096
MA 4,a,252,v,200
RD v,212,241,4096
RD a,72,247,2048
MC 136,a,32,a,112,-4.0
EX 132
MA 136,a,36,a,116
EX 133
MA 136,a,40,a,120
EX 134
MA 136,a,44,a,124
EX 135
MC 140,a,48,a,112,-4.0
CV 182,132,133
CV 183,134,135
MA 140,a,52,a,116
WR 238,180,12288
MA 140,a,56,a,120
FM 30,254,255
FM 31,254,255
MA 140,a,60,a,124
CV 36,24,25
CV 37,26,27
MA 8,a,224,v,188
s_waitcnt lgkmcnt(2)
EX 136
MA 8,a,232,v,204
EX 137
v_smfmac_f32_16x16x64_bf16 v[216:219], v[220:223], v[176:183], v234
s_barrier
MA 8,a,240,v,208
EX 138
MA 8,a,248,v,212
EX 139
MA 12,a,228,v,188
CV 188,136,137
CV 189,138,139
MA 12,a,236,v,204
RD v,192,239,8192
RD v,196,240,8192
MA 12,a,244,v,208
RD v,200,241,8192
RD a,76,247,2560
MA 12,a,252,v,212
RD v,252,243,0
CV 38,28,29
MC 128,v,96,a,96,-4.0
EX 140
MA 128,v,100,a,100
EX 141
MA 128,v,104,a,104
EX 142
MA 128,v,108,a,108
EX 143
MR 16,a,128,v,176,40
s_waitcnt lgkmcnt(3)
CV 190,140,141
CV 191,142,143
MA 16,a,136,v,192
WR 238,188,4096
MA 16,a,144,v,196
s_waitcnt lgkmcnt(1)
v_add_f32 v252, v252, v253
v_add_f32 v254, v254, v255
MA 16,a,152,v,200
CV 39,30,31
v_add_f32 v44, v252, v254
MC 132,v,112,a,96,-4.0
EX 128
MA 132,v,116,a,100
EX 129
MA 132,v,120,a,104
EX 130
MA 132,v,124,a,108
EX 131
MR 20,a,132,v,176,40
CV 176,128,129
CV 177,130,131
MA 20,a,140,v,192
buffer_store_dwordx4 v[32:35], v244, s[48:51], s62 offen offset:0 sc1 nt
MA 20,a,148,v,196
RD v,204,239,0
RD v,208,240,0
MA 20,a,156,v,200
RD v,212,241,0
)"
R"(RD a,80,247,4096
MC 136,v,96,a,112,-4.0
EX 132
MA 136,v,100,a,116
EX 133
MA 136,v,104,a,120
EX 134
MA 136,v,108,a,124
EX 135
MC 140,v,112,a,112,-4.0
CV 178,132,133
CV 179,134,135
MA 140,v,116,a,116
WR 238,176,16384
MA 140,v,120,a,120
v_rcp_f32 v253, v44
MA 140,v,124,a,124
v_maximum3_f32 v246, v246, v44, v45
v_mov_b32 v254, v253
MR 24,a,128,v,184,40
s_waitcnt lgkmcnt(2)
EX 136
MA 24,a,136,v,204
EX 137
v_smfmac_f32_16x16x64_bf16 v[216:219], v[224:227], v[184:191], v234
s_barrier
MA 24,a,144,v,208
EX 138
MA 24,a,152,v,212
EX 139
MR 28,a,132,v,184,40
CV 184,136,137
CV 185,138,139
MA 28,a,140,v,204
RD v,192,239,12288
RD v,196,240,12288
MA 28,a,148,v,208
RD v,200,241,12288
RD a,84,247,4608
MA 28,a,156,v,212
v_permlane32_swap_b32 v253, v254
MC 128,v,144,a,96,-4.0
EX 140
MA 128,v,148,a,100
EX 141
MA 128,v,152,a,104
EX 142
MA 128,v,156,a,108
EX 143
MA 16,a,160,v,180
s_waitcnt lgkmcnt(2)
CV 186,140,141
CV 187,142,143
MA 16,a,168,v,192
WR 238,184,0
MA 16,a,176,v,196
s_waitcnt lgkmcnt(2)
v_mul_f32 v252, -v40, v253
FM 0,253,252
MA 16,a,184,v,200
FM 1,253,252
FM 2,253,252
MC 132,v,160,a,96,-4.0
EX 128
MA 132,v,164,a,100
EX 129
MA 132,v,168,a,104
EX 130
MA 132,v,172,a,108
EX 131
MA 20,a,164,v,180
CV 180,128,129
CV 181,130,131
MA 20,a,172,v,192
buffer_store_dwordx4 v[36:39], v245, s[48:51], s62 offen offset:0 sc1 nt
MA 20,a,180,v,196
RD v,204,239,4096
RD v,208,240,4096
MA 20,a,188,v,200
RD v,212,241,4096
RD a,88,247,6144
MC 136,v,144,a,112,-4.0
EX 132
MA 136,v,148,a,116
EX 133
MA 136,v,152,a,120
EX 134
MA 136,v,156,a,124
EX 135
MC 140,v,160,a,112,-4.0
CV 182,132,133
CV 183,134,135
MA 140,v,164,a,116
WR 238,180,20480
MA 140,v,168,a,120
FM 3,253,252
FM 4,253,252
MA 140,v,172,a,124
FM 5,253,252
FM 6,253,252
MA 24,a,160,v,188
s_waitcnt lgkmcnt(2)
EX 136
MA 24,a,168,v,204
EX 137
v_smfmac_f32_16x16x64_bf16 v[216:219], v[220:223], v[176:183], v234
s_waitcnt vmcnt(14)
s_barrier
MA 24,a,176,v,208
EX 138
MA 24,a,184,v,212
EX 139
MA 28,a,164,v,188
CV 188,136,137
CV 189,138,139
MA 28,a,172,v,204
RD v,192,239,16384
RD v,196,240,16384
MA 28,a,180,v,208
s_add_u32 m0, s61, 0
RD v,200,241,16384
RD a,92,247,6656
MA 28,a,188,v,212
s_add_u32 s72, s72, -1
buffer_load_dwordx4 v237, s[36:39], s60 offen offset:0 lds
s_cbranch_scc1 LOOP2_%=
s_branch TAIL%=
.p2align 8
VER3_%=:
.p2align 6
LOOP3_%=:
MC 128,a,0,a,64,-4.0
s_add_u32 s62, s62, 0x2000
EX 140
MA 128,a,4,a,68
EX 141
MA 128,a,8,a,72
EX 142
MA 128,a,12,a,76
EX 143
MA 16,a,192,v,176
s_waitcnt lgkmcnt(2)
CV 190,140,141
CV 191,142,143
MA 16,a,200,v,192
WR 238,188,4096
MA 16,a,208,v,196
s_waitcnt lgkmcnt(1)
FM 7,253,252
CV 32,0,1
MA 16,a,216,v,200
CV 33,2,3
CV 34,4,5
MC 132,a,16,a,64,-4.0
EX 128
MA 132,a,20,a,68
EX 129
MA 132,a,24,a,72
EX 130
MA 132,a,28,a,76
EX 131
MA 20,a,196,v,176
s_add_u32 m0, s61, 0x780
CV 176,128,129
CV 177,130,131
MA 20,a,204,v,192
buffer_load_dwordx4 v237, s[36:39], s60 offen offset:128 lds
MA 20,a,212,v,196
RD v,204,239,0
RD v,208,240,0
MA 20,a,220,v,200
RD v,212,241,0
RD a,96,247,8192
MC 136,a,0,a,80,-4.0
s_add_u32 s55, s61, 0x2000
EX 132
MA 136,a,4,a,84
s_cmp_lt_u32 s55, s66
EX 133
MA 136,a,8,a,88
s_cselect_b32 s61, s55, s64
EX 134
MA 136,a,12,a,92
s_add_u32 s60, s60, 0x2000
EX 135
MC 140,a,16,a,80,-4.0
CV 178,132,133
CV 179,134,135
MA 140,a,20,a,84
WR 238,176,8192
MA 140,a,24,a,88
CV 35,6,7
v_mul_f32 v255, -v40, v254
MA 140,a,28,a,92
FM 8,254,255
FM 9,254,255
MA 24,a,192,v,184
s_waitcnt lgkmcnt(2)
EX 136
MA 24,a,200,v,204
EX 137
MA 24,a,208,v,208
EX 138
v_smfmac_f32_16x16x64_bf16 v[216:219], v[224:227], v[184:191], v234
s_barrier
MA 24,a,216,v,212
EX 139
MA 28,a,196,v,184
CV 184,136,137
CV 185,138,139
MA 28,a,204,v,204
RD v,192,239,20480
RD v,196,240,20480
MA 28,a,212,v,208
RD v,200,241,20480
RD a,100,247,8704
MA 28,a,220,v,212
v_add_f32 v235, v216, v217
v_mov_b64 v[216:217], 0
MC 128,a,32,a,64,-4.0
EX 140
MA 128,a,36,a,68
EX 141
MA 128,a,40,a,72
EX 142
MA 128,a,44,a,76
EX 143
MA 16,a,224,v,180
s_waitcnt lgkmcnt(2)
CV 186,140,141
CV 187,142,143
MA 16,a,232,v,192
WR 238,184,0
MA 16,a,240,v,196
s_waitcnt lgkmcnt(2)
FM 10,254,255
FM 11,254,255
MA 16,a,248,v,200
FM 12,254,255
FM 13,254,255
MC 132,a,48,a,64,-4.0
EX 128
MA 132,a,52,a,68
EX 129
MA 132,a,56,a,72
EX 130
MA 132,a,60,a,76
EX 131
MA 20,a,228,v,180
CV 180,128,129
CV 181,130,131
MA 20,a,236,v,192
ds_write_b32 v242, v235 offset:512
MA 20,a,244,v,196
RD v,204,239,4096
RD v,208,240,4096
MA 20,a,252,v,200
RD v,212,241,4096
RD a,104,247,10240
MC 136,a,32,a,80,-4.0
EX 132
MA 136,a,36,a,84
EX 133
MA 136,a,40,a,88
EX 134
MA 136,a,44,a,92
EX 135
MC 140,a,48,a,80,-4.0
CV 182,132,133
CV 183,134,135
MA 140,a,52,a,84
WR 238,180,12288
MA 140,a,56,a,88
FM 14,254,255
FM 15,254,255
MA 140,a,60,a,92
CV 36,8,9
CV 37,10,11
MA 24,a,224,v,188
s_waitcnt lgkmcnt(2)
EX 136
MA 24,a,232,v,204
EX 137
MA 24,a,240,v,208
EX 138
v_smfmac_f32_16x16x64_bf16 v[216:219], v[220:223], v[176:183], v234
s_barrier
MA 24,a,248,v,212
EX 139
MA 28,a,228,v,188
CV 188,136,137
CV 189,138,139
MA 28,a,236,v,204
RD v,192,239,8192
RD v,196,240,8192
MA 28,a,244,v,208
RD v,200,241,8192
RD a,108,247,10752
MA 28,a,252,v,212
RD v,252,243,512
CV 38,12,13
MC 128,v,96,a,64,-4.0
EX 140
MA 128,v,100,a,68
EX 141
MA 128,v,104,a,72
EX 142
MA 128,v,108,a,76
EX 143
MR 0,a,128,v,176,40
s_waitcnt lgkmcnt(3)
CV 190,140,141
CV 191,142,143
MA 0,a,136,v,192
WR 238,188,4096
MA 0,a,144,v,196
s_waitcnt lgkmcnt(1)
v_add_f32 v252, v252, v253
v_add_f32 v254, v254, v255
MA 0,a,152,v,200
CV 39,14,15
v_add_f32 v45, v252, v254
MC 132,v,112,a,64,-4.0
EX 128
MA 132,v,116,a,68
EX 129
MA 132,v,120,a,72
EX 130
MA 132,v,124,a,76
EX 131
MR 4,a,132,v,176,40
CV 176,128,129
CV 177,130,131
MA 4,a,140,v,192
buffer_store_dwordx4 v[32:35], v244, s[48:51], s62 offen offset:0 sc1 nt
MA 4,a,148,v,196
RD v,204,239,0
RD v,208,240,0
MA 4,a,156,v,200
RD v,212,241,0
RD a,112,247,12288
MC 136,v,96,a,80,-4.0
EX 132
MA 136,v,100,a,84
EX 133
MA 136,v,104,a,88
EX 134
MA 136,v,108,a,92
EX 135
MC 140,v,112,a,80,-4.0
CV 178,132,133
CV 179,134,135
MA 140,v,116,a,84
WR 238,176,16384
MA 140,v,120,a,88
v_rcp_f32 v253, v45
MA 140,v,124,a,92
v_minimum3_f32 v46, v46, v44, v45
v_mov_b32 v254, v253
MR 8,a,128,v,184,40
s_waitcnt lgkmcnt(2)
EX 136
MA 8,a,136,v,204
EX 137
MA 8,a,144,v,208
EX 138
v_smfmac_f32_16x16x64_bf16 v[216:219], v[224:227], v[184:191], v234
s_barrier
MA 8,a,152,v,212
EX 139
MR 12,a,132,v,184,40
CV 184,136,137
CV 185,138,139
MA 12,a,140,v,204
RD v,192,239,12288
RD v,196,240,12288
MA 12,a,148,v,208
RD v,200,241,12288
RD a,116,247,12800
MA 12,a,156,v,212
v_permlane32_swap_b32 v253, v254
MC 128,v,144,a,64,-4.0
EX 140
MA 128,v,148,a,68
EX 141
MA 128,v,152,a,72
EX 142
MA 128,v,156,a,76
EX 143
MA 0,a,160,v,180
s_waitcnt lgkmcnt(2)
CV 186,140,141
CV 187,142,143
v_mul_f32 v252, -v40, v253
MA 0,a,168,v,192
WR 238,184,0
MA 0,a,176,v,196
s_waitcnt lgkmcnt(2)
FM 16,253,252
FM 17,253,252
)"
R"(MA 0,a,184,v,200
FM 18,253,252
FM 19,253,252
MC 132,v,160,a,64,-4.0
EX 128
MA 132,v,164,a,68
EX 129
MA 132,v,168,a,72
EX 130
MA 132,v,172,a,76
EX 131
MA 4,a,164,v,180
CV 180,128,129
CV 181,130,131
MA 4,a,172,v,192
buffer_store_dwordx4 v[36:39], v245, s[48:51], s62 offen offset:0 sc1 nt
MA 4,a,180,v,196
RD v,204,239,4096
RD v,208,240,4096
MA 4,a,188,v,200
RD v,212,241,4096
RD a,120,247,14336
MC 136,v,144,a,80,-4.0
EX 132
MA 136,v,148,a,84
EX 133
MA 136,v,152,a,88
EX 134
MA 136,v,156,a,92
EX 135
MC 140,v,160,a,80,-4.0
CV 182,132,133
CV 183,134,135
MA 140,v,164,a,84
WR 238,180,20480
MA 140,v,168,a,88
FM 20,253,252
FM 21,253,252
MA 140,v,172,a,92
FM 22,253,252
FM 23,253,252
MA 8,a,160,v,188
s_waitcnt lgkmcnt(2)
EX 136
MA 8,a,168,v,204
EX 137
MA 8,a,176,v,208
EX 138
v_smfmac_f32_16x16x64_bf16 v[216:219], v[220:223], v[176:183], v234
s_waitcnt vmcnt(12)
s_barrier
MA 8,a,184,v,212
EX 139
MA 12,a,164,v,188
CV 188,136,137
CV 189,138,139
MA 12,a,172,v,204
RD v,192,239,16384
RD v,196,240,16384
MA 12,a,180,v,208
s_add_u32 m0, s61, 0
RD v,200,241,16384
RD a,124,247,14848
MA 12,a,188,v,212
buffer_load_dwordx4 v237, s[36:39], s60 offen offset:0 lds
MC 128,a,0,a,96,-4.0
s_add_u32 s62, s62, 0x2000
EX 140
MA 128,a,4,a,100
EX 141
MA 128,a,8,a,104
EX 142
MA 128,a,12,a,108
EX 143
MA 0,a,192,v,176
s_waitcnt lgkmcnt(2)
CV 190,140,141
CV 191,142,143
MA 0,a,200,v,192
WR 238,188,4096
MA 0,a,208,v,196
s_waitcnt lgkmcnt(1)
v_add_u32 v247, s63, v236
CV 32,16,17
MA 0,a,216,v,200
CV 33,18,19
CV 34,20,21
MC 132,a,16,a,96,-4.0
s_add_u32 s55, s63, 0x4000
EX 128
MA 132,a,20,a,100
s_cmp_lt_u32 s55, 0x18000
EX 129
MA 132,a,24,a,104
s_cselect_b32 s63, s55, 0
EX 130
MA 132,a,28,a,108
EX 131
MA 4,a,196,v,176
s_add_u32 m0, s61, 0x780
CV 176,128,129
CV 177,130,131
MA 4,a,204,v,192
buffer_load_dwordx4 v237, s[36:39], s60 offen offset:128 lds
MA 4,a,212,v,196
RD v,204,239,0
RD v,208,240,0
MA 4,a,220,v,200
RD v,212,241,0
RD a,64,247,0
MC 136,a,0,a,112,-4.0
s_add_u32 s55, s61, 0x2000
EX 132
MA 136,a,4,a,116
s_cmp_lt_u32 s55, s66
EX 133
MA 136,a,8,a,120
s_cselect_b32 s61, s55, s64
EX 134
MA 136,a,12,a,124
s_add_u32 s60, s60, 0x2000
EX 135
MC 140,a,16,a,112,-4.0
CV 178,132,133
CV 179,134,135
MA 140,a,20,a,116
WR 238,176,8192
MA 140,a,24,a,120
CV 35,22,23
v_mul_f32 v255, -v40, v254
MA 140,a,28,a,124
FM 24,254,255
FM 25,254,255
MA 8,a,192,v,184
s_waitcnt lgkmcnt(2)
EX 136
MA 8,a,200,v,204
EX 137
MA 8,a,208,v,208
EX 138
v_smfmac_f32_16x16x64_bf16 v[216:219], v[224:227], v[184:191], v234
s_barrier
MA 8,a,216,v,212
EX 139
MA 12,a,196,v,184
CV 184,136,137
CV 185,138,139
MA 12,a,204,v,204
RD v,192,239,20480
RD v,196,240,20480
MA 12,a,212,v,208
RD v,200,241,20480
RD a,68,247,512
MA 12,a,220,v,212
v_add_f32 v235, v216, v217
v_mov_b64 v[216:217], 0
MC 128,a,32,a,96,-4.0
EX 140
MA 128,a,36,a,100
EX 141
MA 128,a,40,a,104
EX 142
MA 128,a,44,a,108
EX 143
MA 0,a,224,v,180
s_waitcnt lgkmcnt(2)
CV 186,140,141
CV 187,142,143
MA 0,a,232,v,192
WR 238,184,0
MA 0,a,240,v,196
s_waitcnt lgkmcnt(2)
FM 26,254,255
FM 27,254,255
MA 0,a,248,v,200
FM 28,254,255
FM 29,254,255
MC 132,a,48,a,96,-4.0
EX 128
MA 132,a,52,a,100
EX 129
MA 132,a,56,a,104
EX 130
MA 132,a,60,a,108
EX 131
MA 4,a,228,v,180
CV 180,128,129
CV 181,130,131
MA 4,a,236,v,192
ds_write_b32 v242, v235 offset:0
MA 4,a,244,v,196
RD v,204,239,4096
RD v,208,240,4096
MA 4,a,252,v,200
RD v,212,241,4096
RD a,72,247,2048
MC 136,a,32,a,112,-4.0
EX 132
MA 136,a,36,a,116
EX 133
MA 136,a,40,a,120
EX 134
MA 136,a,44,a,124
EX 135
MC 140,a,48,a,112,-4.0
CV 182,132,133
CV 183,134,135
MA 140,a,52,a,116
WR 238,180,12288
MA 140,a,56,a,120
FM 30,254,255
FM 31,254,255
MA 140,a,60,a,124
CV 36,24,25
CV 37,26,27
MA 8,a,224,v,188
s_waitcnt lgkmcnt(2)
EX 136
MA 8,a,232,v,204
EX 137
MA 8,a,240,v,208
EX 138
v_smfmac_f32_16x16x64_bf16 v[216:219], v[220:223], v[176:183], v234
s_barrier
MA 8,a,248,v,212
EX 139
MA 12,a,228,v,188
CV 188,136,137
CV 189,138,139
MA 12,a,236,v,204
RD v,192,239,8192
RD v,196,240,8192
MA 12,a,244,v,208
RD v,200,241,8192
RD a,76,247,2560
MA 12,a,252,v,212
RD v,252,243,0
CV 38,28,29
MC 128,v,96,a,96,-4.0
EX 140
MA 128,v,100,a,100
EX 141
MA 128,v,104,a,104
EX 142
MA 128,v,108,a,108
EX 143
MR 16,a,128,v,176,40
s_waitcnt lgkmcnt(3)
CV 190,140,141
CV 191,142,143
MA 16,a,136,v,192
WR 238,188,4096
MA 16,a,144,v,196
s_waitcnt lgkmcnt(1)
v_add_f32 v252, v252, v253
v_add_f32 v254, v254, v255
MA 16,a,152,v,200
CV 39,30,31
v_add_f32 v44, v252, v254
MC 132,v,112,a,96,-4.0
EX 128
MA 132,v,116,a,100
EX 129
MA 132,v,120,a,104
EX 130
MA 132,v,124,a,108
EX 131
MR 20,a,132,v,176,40
CV 176,128,129
CV 177,130,131
MA 20,a,140,v,192
buffer_store_dwordx4 v[32:35], v244, s[48:51], s62 offen offset:0 sc1 nt
MA 20,a,148,v,196
RD v,204,239,0
RD v,208,240,0
MA 20,a,156,v,200
RD v,212,241,0
RD a,80,247,4096
MC 136,v,96,a,112,-4.0
EX 132
MA 136,v,100,a,116
EX 133
MA 136,v,104,a,120
EX 134
MA 136,v,108,a,124
EX 135
MC 140,v,112,a,112,-4.0
CV 178,132,133
CV 179,134,135
MA 140,v,116,a,116
WR 238,176,16384
MA 140,v,120,a,120
v_rcp_f32 v253, v44
MA 140,v,124,a,124
v_maximum3_f32 v246, v246, v44, v45
v_mov_b32 v254, v253
MR 24,a,128,v,184,40
s_waitcnt lgkmcnt(2)
EX 136
MA 24,a,136,v,204
EX 137
MA 24,a,144,v,208
EX 138
v_smfmac_f32_16x16x64_bf16 v[216:219], v[224:227], v[184:191], v234
s_barrier
MA 24,a,152,v,212
EX 139
MR 28,a,132,v,184,40
CV 184,136,137
CV 185,138,139
MA 28,a,140,v,204
RD v,192,239,12288
RD v,196,240,12288
MA 28,a,148,v,208
RD v,200,241,12288
RD a,84,247,4608
MA 28,a,156,v,212
v_permlane32_swap_b32 v253, v254
MC 128,v,144,a,96,-4.0
EX 140
MA 128,v,148,a,100
EX 141
MA 128,v,152,a,104
EX 142
MA 128,v,156,a,108
EX 143
MA 16,a,160,v,180
s_waitcnt lgkmcnt(2)
CV 186,140,141
CV 187,142,143
MA 16,a,168,v,192
WR 238,184,0
MA 16,a,176,v,196
s_waitcnt lgkmcnt(2)
v_mul_f32 v252, -v40, v253
FM 0,253,252
MA 16,a,184,v,200
FM 1,253,252
FM 2,253,252
MC 132,v,160,a,96,-4.0
EX 128
MA 132,v,164,a,100
EX 129
MA 132,v,168,a,104
EX 130
MA 132,v,172,a,108
EX 131
MA 20,a,164,v,180
CV 180,128,129
CV 181,130,131
MA 20,a,172,v,192
buffer_store_dwordx4 v[36:39], v245, s[48:51], s62 offen offset:0 sc1 nt
MA 20,a,180,v,196
RD v,204,239,4096
RD v,208,240,4096
MA 20,a,188,v,200
RD v,212,241,4096
RD a,88,247,6144
MC 136,v,144,a,112,-4.0
EX 132
MA 136,v,148,a,116
EX 133
MA 136,v,152,a,120
EX 134
MA 136,v,156,a,124
EX 135
MC 140,v,160,a,112,-4.0
CV 182,132,133
CV 183,134,135
MA 140,v,164,a,116
WR 238,180,20480
MA 140,v,168,a,120
FM 3,253,252
FM 4,253,252
MA 140,v,172,a,124
FM 5,253,252
FM 6,253,252
MA 24,a,160,v,188
s_waitcnt lgkmcnt(2)
EX 136
MA 24,a,168,v,204
EX 137
MA 24,a,176,v,208
EX 138
v_smfmac_f32_16x16x64_bf16 v[216:219], v[220:223], v[176:183], v234
s_waitcnt vmcnt(14)
s_barrier
MA 24,a,184,v,212
EX 139
MA 28,a,164,v,188
CV 188,136,137
CV 189,138,139
MA 28,a,172,v,204
RD v,192,239,16384
RD v,196,240,16384
MA 28,a,180,v,208
s_add_u32 m0, s61, 0
RD v,200,241,16384
RD a,92,247,6656
MA 28,a,188,v,212
)"
R"(s_add_u32 s72, s72, -1
buffer_load_dwordx4 v237, s[36:39], s60 offen offset:0 lds
s_cbranch_scc1 LOOP3_%=
.p2align 4
TAIL%=:
MA 16,a,192,v,176
s_waitcnt lgkmcnt(2)
s_add_u32 s62, s62, 0x2000
EX 140
MA 16,a,200,v,192
EX 141
MA 16,a,208,v,196
s_waitcnt lgkmcnt(0)
EX 142
MA 16,a,216,v,200
EX 143
MA 20,a,196,v,176
RD v,204,239,0
RD v,208,240,0
MA 20,a,204,v,192
RD v,212,241,0
CV 190,140,141
CV 191,142,143
MA 20,a,212,v,196
WR 238,188,4096
v_smfmac_f32_16x16x64_bf16 v[216:219], v[224:227], v[184:191], v234
FM 7,253,252
CV 32,0,1
CV 33,2,3
CV 34,4,5
CV 35,6,7
MA 20,a,220,v,200
v_mul_f32 v255, -v40, v254
FM 8,254,255
FM 9,254,255
MA 24,a,192,v,184
s_waitcnt lgkmcnt(1)
FM 10,254,255
FM 11,254,255
FM 12,254,255
MA 24,a,200,v,204
FM 13,254,255
FM 14,254,255
FM 15,254,255
MA 24,a,208,v,208
v_add_f32 v235, v216, v217
CV 36,8,9
MA 24,a,216,v,212
ds_write_b32 v242, v235 offset:512
MA 28,a,196,v,184
CV 37,10,11
CV 38,12,13
CV 39,14,15
MA 28,a,204,v,204
buffer_store_dwordx4 v[32:35], v244, s[48:51], s62 offen offset:0 nt
MA 28,a,212,v,208
s_waitcnt lgkmcnt(0)
s_barrier
MA 28,a,220,v,212
RD v,192,239,20480
RD v,196,240,20480
RD v,200,241,20480
RD v,252,243,512
MA 16,a,224,v,180
RD v,64,239,4096
RD v,68,240,4096
RD v,72,241,4096
MA 20,a,228,v,180
buffer_store_dwordx4 v[36:39], v245, s[48:51], s62 offen offset:0 nt
MA 24,a,224,v,188
s_add_u32 s62, s62, 0x2000
MA 28,a,228,v,188
s_waitcnt lgkmcnt(5)
MA 16,a,232,v,192
s_waitcnt lgkmcnt(3)
v_add_f32 v252, v252, v253
v_add_f32 v254, v254, v255
MA 16,a,240,v,196
v_add_f32 v45, v252, v254
MA 16,a,248,v,200
v_rcp_f32 v253, v45
MA 20,a,236,v,192
v_minimum3_f32 v46, v46, v44, v45
v_mov_b32 v254, v253
MA 20,a,244,v,196
MA 20,a,252,v,200
s_waitcnt lgkmcnt(0)
v_permlane32_swap_b32 v253, v254
v_mul_f32 v252, -v40, v253
v_mul_f32 v255, -v40, v254
MA 24,a,232,v,64
MA 24,a,240,v,68
FM 16,253,252
FM 17,253,252
MA 24,a,248,v,72
FM 18,253,252
FM 19,253,252
MA 28,a,236,v,64
FM 20,253,252
FM 21,253,252
FM 22,253,252
MA 28,a,244,v,68
FM 23,253,252
CV 16,16,17
CV 17,18,19
MA 28,a,252,v,72
CV 18,20,21
CV 19,22,23
buffer_store_dwordx4 v[16:19], v244, s[48:51], s62 offen offset:0 nt
FM 24,254,255
FM 25,254,255
FM 26,254,255
FM 27,254,255
s_nop 0
FM 28,254,255
FM 29,254,255
FM 30,254,255
FM 31,254,255
CV 24,24,25
CV 25,26,27
CV 26,28,29
CV 27,30,31
buffer_store_dwordx4 v[24:27], v245, s[48:51], s62 offen offset:0 nt
v_maximum3_f32 v246, v246, v44, v45
v_cmp_nle_f32 vcc, 0x3d000000, v46
s_nop 4
s_mov_b64 s[70:71], vcc
v_cmp_nge_f32 vcc, s69, v246
s_nop 4
s_or_b64 s[70:71], s[70:71], vcc
s_cselect_b32 %[bad], 1, 0
CEND%=:
.purgem MA
.purgem MC
.purgem MR
.purgem EX
.purgem CV
.purgem MU
.purgem FM
.purgem RD
.purgem WR
)"

      : [bad] "=&s"(bad)
      : [ka] "s"(ka), [wid] "s"(wid), [hid] "s"(h)
      : "v0", "v1", "v2", "v3", "v4", "v5", "v6", "v7", "v8", "v9", "v10", "v11", "v12", "v13", "v14", "v15", "v16", "v17", "v18", "v19", "v20", "v21", "v22", "v23", "v24", "v25", "v26", "v27", "v28", "v29", "v30", "v31", "v32", "v33", "v34", "v35", "v36", "v37", "v38", "v39", "v40", "v41", "v42", "v43", "v44", "v45", "v46", "v47", "v48", "v49", "v50", "v51", "v52", "v53", "v54", "v55", "v56", "v57", "v58", "v59", "v60", "v61", "v62", "v63", "v64", "v65", "v66", "v67", "v68", "v69", "v70", "v71", "v72", "v73", "v74", "v75", "v76", "v77", "v78", "v79", "v80", "v81", "v82", "v83", "v84", "v85", "v86", "v87", "v88", "v89", "v90", "v91", "v92", "v93", "v94", "v95", "v96", "v97", "v98", "v99", "v100", "v101", "v102", "v103", "v104", "v105", "v106", "v107", "v108", "v109", "v110", "v111", "v112", "v113", "v114", "v115", "v116", "v117", "v118", "v119", "v120", "v121", "v122", "v123", "v124", "v125", "v126", "v127", "v128", "v129", "v130", "v131", "v132", "v133", "v134", "v135", "v136", "v137", "v138", "v139", "v140", "v141", "v142", "v143", "v144", "v145", "v146", "v147", "v148", "v149", "v150", "v151", "v152", "v153", "v154", "v155", "v156", "v157", "v158", "v159", "v160", "v161", "v162", "v163", "v164", "v165", "v166", "v167", "v168", "v169", "v170", "v171", "v172", "v173", "v174", "v175", "v176", "v177", "v178", "v179", "v180", "v181", "v182", "v183", "v184", "v185", "v186", "v187", "v188", "v189", "v190", "v191", "v192", "v193", "v194", "v195", "v196", "v197", "v198", "v199", "v200", "v201", "v202", "v203", "v204", "v205", "v206", "v207", "v208", "v209", "v210", "v211", "v212", "v213", "v214", "v215", "v216", "v217", "v218", "v219", "v220", "v221", "v222", "v223", "v224", "v225", "v226", "v227", "v228", "v229", "v230", "v231", "v232", "v233", "v234", "v235", "v236", "v237", "v238", "v239", "v240", "v241", "v242", "v243", "v244", "v245", "v246", "v247", "v248", "v249", "v250", "v251", "v252", "v253", "v254", "v255", "a0", "a1", "a2", "a3", "a4", "a5", "a6", "a7", "a8", "a9", "a10", "a11", "a12", "a13", "a14", "a15", "a16", "a17", "a18", "a19", "a20", "a21", "a22", "a23", "a24", "a25", "a26", "a27", "a28", "a29", "a30", "a31", "a32", "a33", "a34", "a35", "a36", "a37", "a38", "a39", "a40", "a41", "a42", "a43", "a44", "a45", "a46", "a47", "a48", "a49", "a50", "a51", "a52", "a53", "a54", "a55", "a56", "a57", "a58", "a59", "a60", "a61", "a62", "a63", "a64", "a65", "a66", "a67", "a68", "a69", "a70", "a71", "a72", "a73", "a74", "a75", "a76", "a77", "a78", "a79", "a80", "a81", "a82", "a83", "a84", "a85", "a86", "a87", "a88", "a89", "a90", "a91", "a92", "a93", "a94", "a95", "a96", "a97", "a98", "a99", "a100", "a101", "a102", "a103", "a104", "a105", "a106", "a107", "a108", "a109", "a110", "a111", "a112", "a113", "a114", "a115", "a116", "a117", "a118", "a119", "a120", "a121", "a122", "a123", "a124", "a125", "a126", "a127", "a128", "a129", "a130", "a131", "a132", "a133", "a134", "a135", "a136", "a137", "a138", "a139", "a140", "a141", "a142", "a143", "a144", "a145", "a146", "a147", "a148", "a149", "a150", "a151", "a152", "a153", "a154", "a155", "a156", "a157", "a158", "a159", "a160", "a161", "a162", "a163", "a164", "a165", "a166", "a167", "a168", "a169", "a170", "a171", "a172", "a173", "a174", "a175", "a176", "a177", "a178", "a179", "a180", "a181", "a182", "a183", "a184", "a185", "a186", "a187", "a188", "a189", "a190", "a191", "a192", "a193", "a194", "a195", "a196", "a197", "a198", "a199", "a200", "a201", "a202", "a203", "a204", "a205", "a206", "a207", "a208", "a209", "a210", "a211", "a212", "a213", "a214", "a215", "a216", "a217", "a218", "a219", "a220", "a221", "a222", "a223", "a224", "a225", "a226", "a227", "a228", "a229", "a230", "a231", "a232", "a233", "a234", "a235", "a236", "a237", "a238", "a239", "a240", "a241", "a242", "a243", "a244", "a245", "a246", "a247", "a248", "a249", "a250", "a251", "a252", "a253", "a254", "a255", "s20", "s21", "s22", "s23", "s24", "s25", "s26", "s27", "s28", "s29", "s30", "s31", "s36", "s37", "s38", "s39", "s40", "s41", "s42", "s43", "s44", "s45", "s46", "s47", "s48", "s49", "s50", "s51", "s52", "s53", "s54", "s55", "s56", "s57", "s58", "s59", "s60", "s61", "s62", "s63", "s64", "s65", "s66", "s67", "s68", "s69", "s70", "s71", "s72", "s73", "s74", "s75", "s76", "s77", "s78", "s79", "vcc", "scc", "memory");
  if (bad) {
    asm volatile("s_waitcnt vmcnt(0)\n\ts_barrier" ::: "memory");
    slow_head(Q + (size_t)h * SQ * HD, K + (size_t)h * SK * HD, V + (size_t)h * SK * HD, O + (size_t)h * SQ * HD, wid);
  }
}

extern "C" void kernel_launch(void* const* d_in, const int* in_sizes, int n_in, void* d_out, int out_size, void* d_ws,
                              size_t ws_size, hipStream_t stream) {
  (void)in_sizes; (void)n_in; (void)out_size; (void)d_ws; (void)ws_size;
  (void)hipFuncSetAttribute((const void*)mha_fwd, hipFuncAttributeMaxDynamicSharedMemorySize, 163840);
  hipLaunchKernelGGL(mha_fwd, dim3(NH), dim3(256), 163840, stream, (const unsigned short*)d_in[0], (const unsigned short*)d_in[1],
                     (const unsigned short*)d_in[2], (unsigned short*)d_out);
}
```
